```python
import torch

def _softmax(x, axis=-1):
    return torch.softmax(x, dim=axis)
def _gelu(x):
    return torch.nn.functional.gelu(x, approximate="tanh")
def _silu(x):
    return torch.nn.functional.silu(x)
def _sigmoid(x):
    return torch.sigmoid(x)
def _layernorm(x, axis=-1, eps=1e-5):
    m = x.mean(dim=axis, keepdim=True)
    v = x.var(dim=axis, keepdim=True, unbiased=False)
    return (x - m) / torch.sqrt(v + eps)
def _scatter_add(tgt, idx, src, axis=0):
    return tgt.clone().index_add_(axis, idx, src)
def _segment_sum(src, seg, n):
    out = torch.zeros((n,) + src.shape[1:], dtype=src.dtype)
    return out.index_add_(0, seg, src)
def _pad(x, pw):
    flat = tuple(v for pair in reversed(pw) for v in pair)
    return torch.nn.functional.pad(x, flat)

kernel_name = 'recurrent_scan__compressed_kv_cla__dense_activation_silu__decode'

def setup_inputs():

    _w = (lambda a: (lambda t: t.to(torch.bfloat16) if t.is_floating_point() else t)(torch.as_tensor(a)))
    def _A_setup():
        B, H, Sq, Sk, D, M = 1, 32, 1, 32768, 128, 512
        Dc = 4 * D
        return {
            "x": _w((lambda *s: torch.randn(*s, dtype=torch.bfloat16) * 0.1)(B, Sq, M)),
            "c_kv": _w((lambda *s: torch.randn(*s, dtype=torch.bfloat16) * 3.2)(B, Sk, Dc)),
            "w_q": _w((lambda *s: torch.randn(*s, dtype=torch.bfloat16) * 0.8)(M, H, D) * (M ** -0.5)),
            "w_uk": _w((lambda *s: torch.randn(*s, dtype=torch.bfloat16) * 0.8)(Dc, H, D) * (Dc ** -0.5)),
            "w_uv": _w((lambda *s: torch.randn(*s, dtype=torch.bfloat16) * 3.2)(Dc, H, D) * (Dc ** -0.5)),
            "w_o": _w((lambda *s: torch.randn(*s, dtype=torch.bfloat16) * 3.2)(H, D, M) * ((H * D) ** -0.5)),
        }
    _A_in = _A_setup()

    def _B_setup():
        B, S, D, F = 1, 1, 512, 1376
        return {
            "x": _w((lambda *s: torch.randn(*s, dtype=torch.bfloat16) * 0.1)(B, S, D)),
            "w1": _w((lambda *s: torch.randn(*s, dtype=torch.bfloat16) * 3.2)(D, F) * (D ** -0.5)),
            "w2": _w((lambda *s: torch.randn(*s, dtype=torch.bfloat16) * 0.8)(F, D) * (F ** -0.5)),
        }
    _B_in = _B_setup()

    return {
        "A_x": _A_in["x"],
        "A_c_kv": _A_in["c_kv"],
        "A_w_q": _A_in["w_q"],
        "A_w_uk": _A_in["w_uk"],
        "A_w_uv": _A_in["w_uv"],
        "A_w_o": _A_in["w_o"],
        "B_x": _B_in["x"],
        "B_w1": _B_in["w1"],
        "B_w2": _B_in["w2"],
    }

def reference(A_x, A_c_kv, A_w_q, A_w_uk, A_w_uv, A_w_o, B_x, B_w1, B_w2):
    def _A_ref(x, c_kv, w_q, w_uk, w_uv, w_o):
        B, Sq, M = x.shape[0], x.shape[1], x.shape[2]
        Sk = c_kv.shape[1]
        H, D = w_q.shape[1], w_q.shape[2]
        q = torch.einsum("bqm,mhd->bhqd", x, w_q)
        k = torch.einsum("bkc,chd->bhkd", c_kv, w_uk)
        v = torch.einsum("bkc,chd->bhkd", c_kv, w_uv)
        scores = torch.einsum("bhqd,bhkd->bhqk", q, k) * (D ** -0.5)
        off = Sk - Sq
        qi = torch.arange(Sq)[:, None]
        kj = torch.arange(Sk)[None, :]
        cmask = kj <= (qi + off)
        scores = torch.where(cmask[None, None], scores, -1e9)
        p = _softmax(scores, axis=-1)
        ctx = torch.einsum("bhqk,bhkd->bhqd", p, v)
        return torch.einsum("bhqd,hdm->bqm", ctx, w_o)

    def _B_ref(x, w1, w2):
        h = torch.einsum("bsd,df->bsf", x, w1)
        h = _silu(h)
        out = torch.einsum("bsf,fd->bsd", h, w2)
        return out

    x = A_x
    xn = _layernorm(x, axis=-1)
    s = _A_ref(xn, A_c_kv, A_w_q, A_w_uk, A_w_uv, A_w_o)
    g = _silu(_B_ref(xn, B_w1, B_w2))
    return x + s * g
```

```cpp
#include <hip/hip_runtime.h>
#include <hip/hip_bf16.h>

typedef unsigned short u16;
__device__ __forceinline__ float bf2f(u16 b) { return __uint_as_float((unsigned)b << 16); }
__device__ __forceinline__ u16 f2bf(float f) { unsigned u = __float_as_uint(f); u += 0x7fffu + ((u >> 16) & 1u); return (u16)(u >> 16); }

constexpr int H = 32, D = 128, M = 512, DC = 512, SK = 32768, F = 1376;

__device__ __forceinline__ void layernorm(const u16* x, float* xn, float* red) {
    int t = threadIdx.x;
    float a = bf2f(x[t]), b = bf2f(x[t + 256]);
    float s = a + b;
    for (int o = 32; o; o >>= 1) s += __shfl_xor(s, o);
    if ((t & 63) == 0) red[t >> 6] = s;
    __syncthreads();
    float mean = (red[0] + red[1] + red[2] + red[3]) * (1.f / 512);
    __syncthreads();
    float da = a - mean, db = b - mean;
    float v = da * da + db * db;
    for (int o = 32; o; o >>= 1) v += __shfl_xor(v, o);
    if ((t & 63) == 0) red[t >> 6] = v;
    __syncthreads();
    float var = (red[0] + red[1] + red[2] + red[3]) * (1.f / 512);
    float r = 1.f / sqrtf(var + 1e-5f);
    xn[t] = da * r; xn[t + 256] = db * r;
    __syncthreads();
}

__global__ void __launch_bounds__(256) k1(const u16* x, const u16* wq, const u16* wuk, const u16* w1, float* qabs, float* hbuf) {
    __shared__ float xn[512], red[4], q[128], qp[2][128];
    layernorm(x, xn, red);
    int t = threadIdx.x, b = blockIdx.x;
    if (b < 32) {
        int h = b, d = t & 127, half = t >> 7;
        float acc = 0;
        for (int m = half * 256; m < half * 256 + 256; ++m) acc += xn[m] * bf2f(wq[(size_t)m * 4096 + h * 128 + d]);
        qp[half][d] = acc;
        __syncthreads();
        if (t < 128) q[t] = qp[0][t] + qp[1][t];
        __syncthreads();
        for (int c = t; c < 512; c += 256) {
            float a = 0;
            for (int dd = 0; dd < 128; ++dd) a += q[dd] * bf2f(wuk[(size_t)c * 4096 + h * 128 + dd]);
            qabs[h * 512 + c] = a * 0.08838834764831845f;
        }
    } else {
        int f = (b - 32) * 256 + t;
        if (f < F) {
            float a = 0;
            for (int m = 0; m < 512; ++m) a += xn[m] * bf2f(w1[(size_t)m * F + f]);
            hbuf[f] = a / (1.f + __expf(-a));
        }
    }
}

__global__ void __launch_bounds__(256) k2(const u16* ckv, const float* qabs, float* po, float* pml) {
    __shared__ float s[32][128];
    __shared__ float mm[32];
    int t = threadIdx.x, b = blockIdx.x;
    const u16* kv = ckv + (size_t)b * 128 * 512;
    for (int i = t; i < 32 * 128; i += 256) {
        int h = i >> 7, k = i & 127;
        float a = 0;
        for (int c = 0; c < 512; ++c) a += qabs[h * 512 + c] * bf2f(kv[k * 512 + c]);
        s[h][k] = a;
    }
    __syncthreads();
    if (t < 32) {
        float m = -1e30f;
        for (int k = 0; k < 128; ++k) m = fmaxf(m, s[t][k]);
        float l = 0;
        for (int k = 0; k < 128; ++k) { float p = __expf(s[t][k] - m); s[t][k] = p; l += p; }
        pml[(b * 32 + t) * 2] = m; pml[(b * 32 + t) * 2 + 1] = l;
    }
    __syncthreads();
    for (int i = t; i < 32 * 512; i += 256) {
        int h = i >> 9, c = i & 511;
        float a = 0;
        for (int k = 0; k < 128; ++k) a += s[h][k] * bf2f(kv[k * 512 + c]);
        po[(size_t)b * 32 * 512 + i] = a;
    }
}

__global__ void __launch_bounds__(256) k3(const float* po, const float* pml, const u16* wuv, float* ctx) {
    __shared__ float w[256], cc[512], red[4];
    int t = threadIdx.x, h = blockIdx.x;
    float m = pml[(t * 32 + h) * 2], l = pml[(t * 32 + h) * 2 + 1];
    float gm = m;
    for (int o = 32; o; o >>= 1) gm = fmaxf(gm, __shfl_xor(gm, o));
    if ((t & 63) == 0) red[t >> 6] = gm;
    __syncthreads();
    gm = fmaxf(fmaxf(red[0], red[1]), fmaxf(red[2], red[3]));
    __syncthreads();
    float e = __expf(m - gm);
    float gl = e * l;
    for (int o = 32; o; o >>= 1) gl += __shfl_xor(gl, o);
    if ((t & 63) == 0) red[t >> 6] = gl;
    __syncthreads();
    gl = red[0] + red[1] + red[2] + red[3];
    w[t] = e / gl;
    __syncthreads();
    for (int c = t; c < 512; c += 256) {
        float a = 0;
        for (int i = 0; i < 256; ++i) a += w[i] * po[(size_t)i * 32 * 512 + h * 512 + c];
        cc[c] = a;
    }
    __syncthreads();
    if (t < 128) {
        float a = 0;
        for (int c = 0; c < 512; ++c) a += cc[c] * bf2f(wuv[(size_t)c * 4096 + h * 128 + t]);
        ctx[h * 128 + t] = a;
    }
}

__global__ void __launch_bounds__(256) k4(const u16* x, const float* ctx, const float* hbuf, const u16* wo, const u16* w2, u16* out) {
    int m = blockIdx.x * 4 + (threadIdx.x >> 6), lane = threadIdx.x & 63;
    float s = 0, y = 0;
    for (int i = lane; i < 4096; i += 64) s += ctx[i] * bf2f(wo[(size_t)i * 512 + m]);
    for (int f = lane; f < F; f += 64) y += hbuf[f] * bf2f(w2[(size_t)f * 512 + m]);
    for (int o = 32; o; o >>= 1) { s += __shfl_xor(s, o); y += __shfl_xor(y, o); }
    if (lane == 0) out[m] = f2bf(bf2f(x[m]) + s * (y / (1.f + __expf(-y))));
}

extern "C" void kernel_launch(void* const* d_in, const int* in_sizes, int n_in, void* d_out, int out_size, void* d_ws, size_t ws_size, hipStream_t stream) {
    const u16* x = (const u16*)d_in[0];
    const u16* ckv = (const u16*)d_in[1];
    const u16* wq = (const u16*)d_in[2];
    const u16* wuk = (const u16*)d_in[3];
    const u16* wuv = (const u16*)d_in[4];
    const u16* wo = (const u16*)d_in[5];
    const u16* w1 = (const u16*)d_in[7];
    const u16* w2 = (const u16*)d_in[8];
    float* ws = (float*)d_ws;
    float* qabs = ws; float* po = qabs + 32 * 512; float* pml = po + 256 * 32 * 512; float* hbuf = pml + 256 * 64; float* ctx = hbuf + 2048;
    k1<<<32 + 6, 256, 0, stream>>>(x, wq, wuk, w1, qabs, hbuf);
    k2<<<256, 256, 0, stream>>>(ckv, qabs, po, pml);
    k3<<<32, 256, 0, stream>>>(po, pml, wuv, ctx);
    k4<<<128, 256, 0, stream>>>(x, ctx, hbuf, wo, w2, (u16*)d_out);
}
```

```cpp
#include <hip/hip_runtime.h>
#include <hip/hip_bf16.h>

#define STAMPS 0
#define DMA_NT 1
#define W1 7
#define W2 11
#define SLP1 4
#define SLP2 6
#define SLP3 12
#define SLP5 6
#define NOSTREAM 0
#define PSLP 0
#define CODE_KB 12

#define GAS __attribute__((address_space(1)))
#define LAS __attribute__((address_space(3)))
#define DI __device__ __forceinline__
typedef unsigned v4u __attribute__((ext_vector_type(4)));
typedef unsigned v2u __attribute__((ext_vector_type(2)));
typedef float f32x4 __attribute__((ext_vector_type(4)));
typedef float f32x2 __attribute__((ext_vector_type(2)));
typedef short s16x4 __attribute__((ext_vector_type(4)));
typedef short s16x8 __attribute__((ext_vector_type(8)));
typedef __bf16 bf16x8 __attribute__((ext_vector_type(8)));
typedef __bf16 bf16x2 __attribute__((ext_vector_type(2)));
typedef unsigned long long u64;
typedef unsigned short u16;

namespace {
constexpr int F = 1376;
constexpr int SC1 = 16;
constexpr int RSRC_W3 = 0x00020000;
constexpr unsigned SPIN_CAP = 1u << 16;

constexpr int WS_E = 0;
constexpr int WS_QP = 4096;
constexpr int WS_QA = WS_QP + 256 * 1024;
constexpr int WS_HP = WS_QA + 256 * 256;
constexpr int WS_CP = WS_HP + 256 * 512;
constexpr int WS_FP = WS_CP + 256 * 1024;
constexpr int OSTR = 18 * 1024 + 256;
constexpr int WS_OP = WS_FP + 256 * 1024;
constexpr int WS_END = WS_OP + 256 * OSTR;
constexpr int WS_DBG = 8 << 20;

constexpr int L_RING = 0;
constexpr int L_WV = 0;
constexpr int L_WO = 16384;
constexpr int L_W2 = 32768;
constexpr int L_W1 = 40960;
constexpr int L_Q = 131072;
constexpr int L_PX = L_Q + 16384;
constexpr int L_MX = L_PX + 2048;
constexpr int L_VF = L_MX + 256;
constexpr int L_QV = L_VF + 64;
constexpr int L_PART = L_QV + 256;
constexpr int L_ML = L_PART + 1024;
constexpr int L_HV = L_ML + 64;
constexpr int L_JUNK = L_HV + 512;
constexpr int L_END = L_JUNK + 1024;
static_assert(L_END <= 160 * 1024 && L_JUNK % 16 == 0, "lds");

DI float bflo(unsigned u) { return __uint_as_float(u << 16); }
DI float bfhi(unsigned u) { return __uint_as_float(u & 0xffff0000u); }
DI unsigned pkbf(float a, float b) { f32x2 f = {a, b}; return __builtin_bit_cast(unsigned, __builtin_convertvector(f, bf16x2)); }
template <int C> DI float dpp(float v) { return __builtin_amdgcn_update_dpp(0.f, v, C, 0xf, 0xf, true); }
template <int C> DI unsigned dppu(unsigned v) { return (unsigned)__builtin_amdgcn_update_dpp(0, (int)v, C, 0xf, 0xf, true); }
constexpr int ROR1 = 0x121, ROR2 = 0x122, ROR4 = 0x124, ROR8 = 0x128, QX1 = 0xB1, QX2 = 0x4E, HMIR = 0x141;
DI float sw32add(float a, float b) { auto r = __builtin_amdgcn_permlane32_swap(__float_as_uint(a), __float_as_uint(b), false, false); return __uint_as_float(r[0]) + __uint_as_float(r[1]); }
DI float sw16add(float a, float b) { auto r = __builtin_amdgcn_permlane16_swap(__float_as_uint(a), __float_as_uint(b), false, false); return __uint_as_float(r[0]) + __uint_as_float(r[1]); }
DI float vmax(float a, float b) { float r; asm("v_max_f32 %0, %1, %2" : "=v"(r) : "v"(a), "v"(b)); return r; }
DI float vmax3(float a, float b, float c) { float r; asm("v_max3_f32 %0, %1, %2, %3" : "=v"(r) : "v"(a), "v"(b), "v"(c)); return r; }
DI float sw32max(float a) { auto r = __builtin_amdgcn_permlane32_swap(__float_as_uint(a), __float_as_uint(a), false, false); return fmaxf(__uint_as_float(r[0]), __uint_as_float(r[1])); }
DI float sw16max(float a) { auto r = __builtin_amdgcn_permlane16_swap(__float_as_uint(a), __float_as_uint(a), false, false); return fmaxf(__uint_as_float(r[0]), __uint_as_float(r[1])); }
DI float wave_sum(float v) { v += dpp<ROR8>(v); v += dpp<ROR4>(v); v += dpp<ROR2>(v); v += dpp<ROR1>(v); v = sw16add(v, v); return sw32add(v, v); }
DI float ex2(float v) { return __builtin_amdgcn_exp2f(v); }
DI float silu(float v) { return v * __builtin_amdgcn_rcpf(1.f + ex2(v * -1.4426950408889634f)); }
DI f32x4 mfma(v4u a, v4u b, f32x4 c) { return __builtin_amdgcn_mfma_f32_16x16x32_bf16(__builtin_bit_cast(bf16x8, a), __builtin_bit_cast(bf16x8, b), c, 0, 0, 0); }
DI v4u trrd(LAS char* p0, LAS char* p1) {
    const s16x4 lo = __builtin_amdgcn_ds_read_tr16_b64_v4i16((LAS s16x4*)p0), hi = __builtin_amdgcn_ds_read_tr16_b64_v4i16((LAS s16x4*)p1);
    return __builtin_bit_cast(v4u, __builtin_shufflevector(lo, hi, 0, 1, 2, 3, 4, 5, 6, 7));
}
#define BAR() do { asm volatile("s_waitcnt lgkmcnt(0)" ::: "memory"); __builtin_amdgcn_s_barrier(); asm volatile("" ::: "memory"); } while (0)
#define VMW(n) asm volatile("s_waitcnt vmcnt(%0)" :: "i"(n) : "memory")
#define CBAR() asm volatile("" ::: "memory")
#define SB() __builtin_amdgcn_sched_barrier(0)
#define ST(k) do {} while (0)
#define STOUT() do {} while (0)
#define STV(k, v) (void)(v)
#define NTS " nt"
DI void dma16(unsigned m0v, unsigned voff, const char* base) {
    asm volatile("s_mov_b32 m0, %0\n\ts_nop 0\n\tglobal_load_lds_dwordx4 %1, %2" :: "s"(m0v), "v"(voff), "s"(base) : "memory");
}
template <int W, int R0, int R1> DI void issue_rows(const char* gbase, unsigned lds0, unsigned vbase) {
#pragma unroll
    for (int r = R0; r < R1; ++r) {
        const int i = r & 15, n = r >> 4;
        const unsigned voff = vbase ^ ((i << 4) | ((i >> 2) << 12) | (n << 16));
        if (r == R0 || (i & 3) == 0) {
            const unsigned m0v = lds0 + ((n & 1) << 16) + ((i >> 2) << 12);
            asm volatile("s_mov_b32 m0, %0\n\ts_waitcnt vmcnt(%3)\n\tglobal_load_lds_dwordx4 %1, %2 offset:%4" NTS
                         :: "s"(m0v), "v"(voff), "s"(gbase), "i"(W - 1), "i"((i & 3) * 1024) : "memory");
        } else {
            asm volatile("s_waitcnt vmcnt(%2)\n\tglobal_load_lds_dwordx4 %0, %1 offset:%3" NTS
                         :: "v"(voff), "s"(gbase), "i"(W - 1), "i"((i & 3) * 1024) : "memory");
        }
    }
}
DI unsigned gather8(__amdgpu_buffer_rsrc_t rs, int off, unsigned tag, LAS char* dst) {
    float c0, c1; unsigned spin;
    for (spin = 0;; ++spin) {
        CBAR();
        const v4u g0 = __builtin_amdgcn_raw_buffer_load_b128(rs, off, 0, SC1), g1 = __builtin_amdgcn_raw_buffer_load_b128(rs, off, 4096, SC1);
        const unsigned bad = ((g0.y ^ tag) | (g0.w ^ tag)) | ((g1.y ^ tag) | (g1.w ^ tag));
        c0 = __uint_as_float(g0.x) + __uint_as_float(g1.x); c1 = __uint_as_float(g0.z) + __uint_as_float(g1.z);
        if (__builtin_amdgcn_ballot_w64(bad != 0) == 0 || spin > SPIN_CAP) break;
        if (PSLP) __builtin_amdgcn_s_sleep(PSLP);
    }
    float cx = sw32add(c0, c1);
    cx = sw16add(cx, cx);
    *(LAS u16*)dst = (u16)pkbf(cx, cx);
    return spin;
}
}

__global__ void __launch_bounds__(512, 2)
mla_kernel(const u16* __restrict__ x, const char* __restrict__ ckv, const char* __restrict__ wq, const char* __restrict__ wuk,
           const char* __restrict__ wuv, const char* __restrict__ wo, const char* __restrict__ w1, const char* __restrict__ w2,
           unsigned* __restrict__ out, char* ws) {
    extern __shared__ __attribute__((aligned(16))) char smem_[];
    LAS char* L = (LAS char*)smem_;
    const char* pc;
    asm volatile("s_getpc_b64 %0" : "=s"(pc));
    pc = (const char*)((size_t)pc & ~(size_t)255);
    const int lane = threadIdx.x & 63, w = __builtin_amdgcn_readfirstlane(threadIdx.x >> 6), wa = w & 3;
    const bool isA = w >= 4;
    asm volatile("" :: "s"(x), "s"(ckv), "s"(wq), "s"(wuk), "s"(wuv), "s"(wo), "s"(w1), "s"(w2), "s"(out), "s"(ws));
    ST(0);
    const int b = blockIdx.x, xcd = b & 7, i8 = b >> 3, a = i8 & 1, ip = i8 >> 1;
    const int p = xcd * 16 + ip;
    const int hh = ip, h = 16 * a + hh, j = xcd;
    const int h2 = 4 * xcd + (i8 >> 3), j2 = i8 & 7;
    const int g = lane >> 4, rho = lane & 15;
    const unsigned lbase = (unsigned)(size_t)L;

    unsigned e0; v4u xv, xf0, xf1;
    {
        const unsigned o1 = lane * 16, o2 = 128 * j + 16 * g;
        asm volatile("global_load_dword %0, %4, %5 sc1\n\tglobal_load_dwordx4 %1, %6, %7\n\tglobal_load_dwordx4 %2, %8, %7\n\tglobal_load_dwordx4 %3, %8, %7 offset:64"
                     : "=&v"(e0), "=&v"(xv), "=&v"(xf0), "=&v"(xf1) : "v"(0), "s"(ws), "v"(o1), "s"(x), "v"(o2) : "memory");
    }
    const size_t wrow = ((size_t)(64 * j) * 4096 + 128 * h + 32 * wa) * 2;
    const unsigned wvoff = (lane >> 2) * 8192 + (((lane & 3) ^ ((lane >> 4) & 2)) << 4);
    const char* gbase = ckv + ((size_t)p * 256 + wa * 16) * 1024;
    {
        const char* sb = isA ? gbase : wq + wrow;
        const unsigned vb = isA ? (unsigned)lane << 4 : wvoff, xm = isA ? 16u : 0u, gs = isA ? 1024u : 131072u;
        const unsigned lb = lbase + (isA ? L_RING + wa * 16384 : L_Q + wa * 4096);
#pragma unroll
        for (int i = 0; i < 4; ++i) dma16(lb + i * 1024, (vb ^ (i * xm)) + i * gs, sb);
        dma16(lbase + L_JUNK, (unsigned)lane << 4, pc + ((w + 8 * (i8 & 1)) % CODE_KB) * 1024);
    }
    if (threadIdx.x < 8) *(volatile LAS unsigned*)(L + L_VF + 4 * lane) = 0u;
    BAR();
    const __amdgpu_buffer_rsrc_t rs = __builtin_amdgcn_make_buffer_rsrc(ws, 0, WS_END, RSRC_W3);
    const v4u ones = {0x3f803f80u, 0x3f803f80u, 0x3f803f80u, 0x3f803f80u};
    const int q4 = rho >> 2, p4 = lane & 3;
    const unsigned tb1 = wa * 4096 + (8 * g + q4) * 64 + (((p4 >> 1) ^ (2 * (g & 1))) << 4) + (p4 & 1) * 8;
    unsigned tag; float mean, rstd;
#define LN_STATS(NY) do { \
        asm volatile("s_waitcnt vmcnt(%4)" : "+v"(e0), "+v"(xv), "+v"(xf0), "+v"(xf1) : "i"(NY) : "memory"); \
        const float xf[8] = {bflo(xv.x), bfhi(xv.x), bflo(xv.y), bfhi(xv.y), bflo(xv.z), bfhi(xv.z), bflo(xv.w), bfhi(xv.w)}; \
        mean = wave_sum(((xf[0] + xf[1]) + (xf[2] + xf[3])) + ((xf[4] + xf[5]) + (xf[6] + xf[7]))) * (1.f / 512); \
        float vs = 0.f; \
        _Pragma("unroll") for (int i = 0; i < 8; ++i) { const float dd = xf[i] - mean; vs += dd * dd; } \
        rstd = __builtin_amdgcn_rsqf(wave_sum(vs) * (1.f / 512) + 1e-5f); \
        tag = __builtin_amdgcn_readfirstlane(e0) + 1u; \
        xf0 = rho == 0 ? xf0 : ones; xf1 = rho == 0 ? xf1 : ones; \
    } while (0)
    LN_STATS(5);

    if (!isA) {
        v4u wk[4];
        {
            const char* kb = wuk + ((size_t)(64 * j + 16 * wa) * 4096 + 128 * h) * 2;
            const unsigned ko = rho * 8192 + g * 16;
            asm volatile("global_load_dwordx4 %0, %4, %5\n\tglobal_load_dwordx4 %1, %4, %5 offset:64\n\tglobal_load_dwordx4 %2, %4, %5 offset:128\n\tglobal_load_dwordx4 %3, %4, %5 offset:192"
                         : "=&v"(wk[0]), "=&v"(wk[1]), "=&v"(wk[2]), "=&v"(wk[3]) : "v"(ko), "s"(kb) : "memory");
        }
        ST(1);
        VMW(5);
        {
            f32x4 c[2];
#pragma unroll
            for (int e = 0; e < 2; ++e) {
                LAS char* t = L + L_Q + (tb1 ^ (e * 32));
                c[e] = mfma(xf0, trrd(t, t + 256), f32x4{0.f, 0.f, 0.f, 0.f});
                c[e] = mfma(xf1, trrd(t + 2048, t + 2304), c[e]);
            }
            const v4u o = {__float_as_uint(rstd * (c[0].x - mean * c[0].y)), tag, __float_as_uint(rstd * (c[1].x - mean * c[1].y)), tag};
            asm volatile("s_waitcnt lgkmcnt(0)" ::: "memory");
            if (lane < 16) __builtin_amdgcn_raw_buffer_store_b128(o, rs, WS_QP + (h * 8 + j) * 1024 + (16 * wa + rho) * 16, 0, SC1);
        }
        ST(2);
        __builtin_amdgcn_s_sleep(SLP1);
        { const unsigned np = gather8(rs, WS_QP + (h * 8 + g) * 1024 + (16 * wa + rho) * 16, tag, L + L_QV + (32 * wa + 16 * (lane >> 5) + rho) * 2); STV(24, np); }
        ST(3);
        if (lane == 0) __hip_atomic_fetch_add((LAS unsigned*)(L + L_VF + 16), 1u, __ATOMIC_RELAXED, __HIP_MEMORY_SCOPE_WORKGROUP);
        {
            v4u qb[4];
            for (unsigned spin = 0;; ++spin) {
                CBAR();
                const unsigned cnt = *(volatile LAS unsigned*)(L + L_VF + 16);
                CBAR();
#pragma unroll
                for (int s = 0; s < 4; ++s) qb[s] = *(LAS v4u*)(L + L_QV + s * 64 + g * 16);
                CBAR();
                if (__builtin_amdgcn_readfirstlane(cnt) >= 4u || spin > SPIN_CAP * 16) break;
            }
            asm volatile("" : "+v"(wk[0]), "+v"(wk[1]), "+v"(wk[2]), "+v"(wk[3]));
            f32x4 cv = {0.f, 0.f, 0.f, 0.f};
#pragma unroll
            for (int s = 0; s < 4; ++s) cv = mfma(wk[s], qb[s], cv);
            cv *= 0.08838834764831845f * 1.4426950408889634f;
            const v4u o = {pkbf(cv.x, cv.y), pkbf(cv.z, cv.w), tag, ~tag};
            if (rho == 0) __builtin_amdgcn_raw_buffer_store_b128(o, rs, WS_QA + (a * 128 + hh * 8 + j) * 256 + (wa * 4 + g) * 16, 0, SC1);
        }
        ST(4);
        const int z = 8 * (g & 1) + 2 * q4 + (g >> 1);
        unsigned ta[8];
#pragma unroll
        for (int t = 0; t < 8; ++t) { ta[t] = lbase + z * 1024 + 256 * wa + 16 * ((2 * t + (p4 >> 1)) ^ z) + 8 * (p4 & 1); asm volatile("" : "+v"(ta[t])); }
        f32x4 acc[8], lacc = {0.f, 0.f, 0.f, 0.f};
#pragma unroll
        for (int t = 0; t < 8; ++t) { acc[t] = f32x4{0.f, 0.f, 0.f, 0.f}; asm volatile("" : "+v"(acc[t])); }
        __builtin_amdgcn_s_sleep(SLP2);
        {
            const int go = WS_QA + (a * 128 + wa * 32) * 256 + lane * 16;
            LAS char* qd = L + L_Q + ((2 * g + (p4 >> 1)) * 64 + q4 * 16 + 4 * wa) * 16 + (p4 & 1) * 8;
            for (unsigned spin = 0;; ++spin) {
                CBAR();
                v4u gq[8];
#pragma unroll
                for (int k = 0; k < 8; ++k) gq[k] = __builtin_amdgcn_raw_buffer_load_b128(rs, go + (k & 3) * 1024, (k >> 2) * 4096, SC1);
                unsigned bad = 0;
#pragma unroll
                for (int k = 0; k < 8; ++k) {
                    bad |= (gq[k].z ^ tag) | (gq[k].w ^ ~tag);
                    *(LAS v2u*)(qd + (k & 1) * 8192 + (k >> 1) * 16) = v2u{gq[k].x, gq[k].y};
                }
                if (__builtin_amdgcn_ballot_w64(bad != 0) == 0 || spin > SPIN_CAP) { STV(25, spin); break; }
                if (PSLP) __builtin_amdgcn_s_sleep(PSLP);
            }
        }
        ST(5);
        BAR();
        float mref = -1e30f;
#pragma nounroll
        for (int n = 0; n < 4; ++n) {
            v4u tv[2][8];
#pragma unroll
            for (int ks = 0; ks < 2; ++ks)
#pragma unroll
                for (int t = 0; t < 8; ++t) tv[ks][t] = trrd((LAS char*)(size_t)ta[t] + ks * 32768, (LAS char*)(size_t)ta[t] + ks * 32768 + 16384);
#pragma unroll
            for (int t = 0; t < 8; ++t) ta[t] ^= 65536u;
            BAR();
            const unsigned vf = *(volatile LAS unsigned*)(L + L_VF + 4 * n);
            v4u pf0 = *(LAS v4u*)(L + L_PX + lane * 16), pf1 = *(LAS v4u*)(L + L_PX + 1024 + lane * 16);
            if (__builtin_amdgcn_readfirstlane(vf) != 0) {
                BAR();
                const f32x4 m4 = *(LAS f32x4*)(L + L_MX + rho * 16);
                const float mn = fmaxf(fmaxf(fmaxf(m4.x, m4.y), fmaxf(m4.z, m4.w)), mref);
                const float al = ex2(mref - mn);
                mref = mn;
#pragma unroll
                for (int t = 0; t < 8; ++t) acc[t] *= al;
                lacc *= al;
                BAR();
                pf0 = *(LAS v4u*)(L + L_PX + lane * 16); pf1 = *(LAS v4u*)(L + L_PX + 1024 + lane * 16);
            }
#pragma unroll
            for (int t = 0; t < 8; ++t) acc[t] = mfma(tv[0][t], pf0, acc[t]);
            lacc = mfma(ones, pf0, lacc);
            lacc = mfma(ones, pf1, lacc);
#pragma unroll
            for (int t = 0; t < 8; ++t) acc[t] = mfma(tv[1][t], pf1, acc[t]);
            if (n < 3) BAR();
        }
        const int ob = WS_OP + (a * 128 + p) * OSTR;
        v4u o[4];
#pragma unroll
        for (int k = 0; k < 4; ++k) {
            const f32x4 t0 = acc[2 * k], t1 = acc[2 * k + 1];
            o[k] = v4u{pkbf(t0.x, t0.y), pkbf(t0.z, t0.w), pkbf(t1.x, t1.y), pkbf(t1.z, t1.w)};
            __builtin_amdgcn_raw_buffer_store_b128(o[k], rs, wa * 4096 + rho * 64 + g * 16 + k * 1024, ob, SC1);
        }
        SB();
        {
            const unsigned c0 = (o[0].x + o[0].y + o[0].z) + (o[0].w + o[1].x + o[1].y) + (o[1].z + o[1].w);
            const unsigned c1 = (o[2].x + o[2].y + o[2].z) + (o[2].w + o[3].x + o[3].y) + (o[3].z + o[3].w);
            auto r1 = __builtin_amdgcn_permlane32_swap(c0, c1, false, false); unsigned s1 = r1[0] + r1[1];
            auto r3 = __builtin_amdgcn_permlane16_swap(s1, s1, false, false); s1 = r3[0] + r3[1];
            const unsigned mb = __float_as_uint(mref), lb = __float_as_uint(lacc.x);
            const v4u hd = {mb, lb, s1 + mb + lb, tag};
            if ((lane & 16) == 0) __builtin_amdgcn_raw_buffer_store_b128(hd, rs, 16384 + (2 * wa + (lane >> 5)) * 256 + rho * 16, ob, SC1);
        }
        ST(10);
        __builtin_amdgcn_s_sleep(SLP3);
    } else {
        const unsigned lds0 = lbase + L_RING + wa * 16384;
        const unsigned vbase = lane << 4;
        ST(1);
        issue_rows<W1, 4, 32>(gbase, lds0, vbase);
        const int kap = 8 * (q4 & 1) + 2 * (rho & 3) + (rho >> 3);
        unsigned sa[4];
#pragma unroll
        for (int s = 0; s < 4; ++s) { sa[s] = lbase + (16 * wa + kap) * 1024 + 16 * ((8 * (s >> 1) + 2 * g + (s & 1)) ^ kap); asm volatile("" : "+v"(sa[s])); }
        LAS v2u* pxw = (LAS v2u*)(L + L_PX + (wa >> 1) * 1024 + lane * 16 + (wa & 1) * 8);
        __builtin_amdgcn_s_setprio(1);
        ST(2);
        BAR();
        ST(3);
        v4u qf[16];
#pragma unroll
        for (int s = 0; s < 16; ++s) qf[s] = *(LAS v4u*)(L + L_Q + s * 1024 + lane * 16);
        float mref = -1e30f;
        const int U0 = (43 * h) >> 3, un = ((43 * (h + 1)) >> 3) - U0;
#pragma nounroll
        for (int n = 0;; ++n) {
            f32x4 sv = {0.f, 0.f, 0.f, 0.f};
            {
                v4u af[16];
#pragma unroll
                for (int s = 0; s < 16; ++s) af[s] = *(LAS v4u*)((LAS char*)(size_t)sa[s & 3] + (s >> 2) * 256);
                SB();
#pragma unroll
                for (int s = 0; s < 16; ++s) sv = mfma(af[s], qf[s], sv);
            }
            f32x4 d = sv - mref;
            *pxw = v2u{pkbf(ex2(d.x), ex2(d.y)), pkbf(ex2(d.z), ex2(d.w))};
            if (!(vmax(vmax3(d.x, d.y, d.z), d.w) <= 10.f)) *(volatile LAS unsigned*)(L + L_VF + 4 * n) = 1u;
            BAR();
            if (__builtin_amdgcn_readfirstlane(*(volatile LAS unsigned*)(L + L_VF + 4 * n)) != 0) {
                float tm = fmaxf(fmaxf(sv.x, sv.y), fmaxf(sv.z, sv.w));
                tm = sw32max(sw16max(tm));
                if (lane < 16) *(LAS float*)(L + L_MX + lane * 16 + wa * 4) = tm;
                BAR();
                const f32x4 m4 = *(LAS f32x4*)(L + L_MX + rho * 16);
                mref = fmaxf(fmaxf(fmaxf(m4.x, m4.y), fmaxf(m4.z, m4.w)), mref);
                d = sv - mref;
                *pxw = v2u{pkbf(ex2(d.x), ex2(d.y)), pkbf(ex2(d.z), ex2(d.w))};
                BAR();
            }
            if (n == 3) break;
#pragma unroll
            for (int s = 0; s < 4; ++s) sa[s] ^= 65536u;
            if (n == 0) { issue_rows<W2, 32, 32 + W2>(gbase, lds0, vbase); VMW(W2); }
            else if (n == 1) { issue_rows<W2, 32 + W2, 64>(gbase, lds0, vbase); }
            else {
#pragma unroll
                for (int i = 0; i < 2; ++i) {
                    const int uc = 2 * wa + (lane & 1);
                    dma16(lbase + L_W1 + wa * 2048 + i * 1024, (32 * i + (lane >> 1)) * (F * 2) + (uc < un ? uc : 0) * 16, w1 + (size_t)(64 * j) * (F * 2) + U0 * 16);
                }
#pragma unroll
                for (int i = 0; i < 4; ++i) dma16(lbase + L_WV + wa * 4096 + i * 1024, wvoff + i * 131072, wuv + wrow);
                const int r8 = lane >> 3;
#pragma unroll
                for (int i = 0; i < 4; ++i)
                    dma16(lbase + L_WO + (4 * wa + i) * 1024, (8 * i + r8) * 1024 + (((lane & 7) ^ ((r8 & 2) + 4 * (i & 1))) << 4), wo + ((size_t)(h2 * 128 + 32 * wa) * 512 + 64 * j2) * 2);
#pragma unroll
                for (int i = 0; i < 2; ++i) {
                    const int k = 2 * wa + i;
                    const int V0 = (43 * h2) >> 3, vn = ((43 * (h2 + 1)) >> 3) - V0;
                    dma16(lbase + L_W2 + k * 1024, r8 * 1024 + (((lane & 7) ^ ((r8 & 2) + 4 * (i & 1))) << 4), w2 + (size_t)(8 * (V0 + (k < vn ? k : 0))) * 1024 + 128 * j2);
                }
                VMW(12);
            }
            BAR();
        }
        __builtin_amdgcn_s_setprio(0);
        VMW(10);
        {
            LAS char* t = L + L_W1 + wa * 2048 + (8 * g + q4) * 32 + p4 * 8;
            f32x4 c = mfma(xf0, trrd(t, t + 128), f32x4{0.f, 0.f, 0.f, 0.f});
            c = mfma(xf1, trrd(t + 1024, t + 1152), c);
            if (lane < 16) __builtin_amdgcn_raw_buffer_store_b64(v2u{__float_as_uint(rstd * (c.x - mean * c.y)), tag}, rs, WS_HP + (h * 8 + j) * 512 + wa * 128 + rho * 8, 0, SC1);
        }
        VMW(1);
        ST(11);
    }
    BAR();
    ST(12);
    v4u bv[4], bo[4], by[2];
    {
        const int msg = lane >> 3, pc8 = lane & 7;
        const int base = WS_OP + (a * 128 + w * 16 + msg) * OSTR;
        const int po = base + (j * 2 + (pc8 >> 2)) * 1024 + hh * 64 + (pc8 & 3) * 16, ho = base + 16384 + j * 256 + hh * 16;
        v4u pl[2], hd[2];
#pragma unroll
        for (int k = 0; k < 2; ++k) {
            pl[k] = __builtin_amdgcn_raw_buffer_load_b128(rs, po, k * 8 * OSTR, SC1);
            hd[k] = __builtin_amdgcn_raw_buffer_load_b128(rs, ho, k * 8 * OSTR, SC1);
        }
        SB();
#pragma unroll
        for (int e = 0; e < 2; ++e) { LAS char* t = L + L_WV + (tb1 ^ (e * 32)); bv[2 * e] = trrd(t, t + 256); bv[2 * e + 1] = trrd(t + 2048, t + 2304); }
        {
            const unsigned cb = (((2 * wa + (p4 >> 1)) ^ ((q4 & 2) + 4 * (g & 1))) << 4) + (p4 & 1) * 8;
            LAS char* t = L + L_WO + (8 * g + q4) * 128 + cb;
#pragma unroll
            for (int s = 0; s < 4; ++s) bo[s] = trrd(t + s * 4096, t + s * 4096 + 512);
            by[0] = trrd(t + (L_W2 - L_WO), t + (L_W2 - L_WO) + 512);
            LAS char* t2 = L + L_W2 + 4096 + (8 * (g & 1) + q4) * 128 + cb;
            by[1] = trrd(t2, t2 + 512);
        }
        SB();
        for (unsigned spin = 0;; ++spin) {
            bool ok[2];
#pragma unroll
            for (int k = 0; k < 2; ++k) {
                unsigned s1 = (pl[k].x + pl[k].y + pl[k].z) + pl[k].w;
                s1 += dppu<QX1>(s1); s1 += dppu<QX2>(s1); s1 += dppu<HMIR>(s1);
                ok[k] = (s1 + hd[k].x + hd[k].y == hd[k].z) & (hd[k].w == tag);
            }
            if (__builtin_amdgcn_ballot_w64(!(ok[0] & ok[1])) == 0 || spin > SPIN_CAP) { STV(26, spin); break; }
            if (PSLP) __builtin_amdgcn_s_sleep(PSLP);
            CBAR();
#pragma unroll
            for (int k = 0; k < 2; ++k) if (!ok[k]) {
                pl[k] = __builtin_amdgcn_raw_buffer_load_b128(rs, po, k * 8 * OSTR, SC1);
                hd[k] = __builtin_amdgcn_raw_buffer_load_b128(rs, ho, k * 8 * OSTR, SC1);
            }
        }
        ST(13);
        const float m0 = __uint_as_float(hd[0].x), m1 = __uint_as_float(hd[1].x);
        float mw = fmaxf(m0, m1);
        mw = fmaxf(mw, dpp<ROR8>(mw)); mw = sw32max(sw16max(mw));
        const float f0 = ex2(m0 - mw), f1 = ex2(m1 - mw);
        float a8[8];
        a8[0] = f0 * bflo(pl[0].x) + f1 * bflo(pl[1].x); a8[1] = f0 * bfhi(pl[0].x) + f1 * bfhi(pl[1].x);
        a8[2] = f0 * bflo(pl[0].y) + f1 * bflo(pl[1].y); a8[3] = f0 * bfhi(pl[0].y) + f1 * bfhi(pl[1].y);
        a8[4] = f0 * bflo(pl[0].z) + f1 * bflo(pl[1].z); a8[5] = f0 * bfhi(pl[0].z) + f1 * bfhi(pl[1].z);
        a8[6] = f0 * bflo(pl[0].w) + f1 * bflo(pl[1].w); a8[7] = f0 * bfhi(pl[0].w) + f1 * bfhi(pl[1].w);
        const float v0 = sw32add(a8[0], a8[4]), v1 = sw32add(a8[1], a8[5]), v2 = sw32add(a8[2], a8[6]), v3 = sw32add(a8[3], a8[7]);
        float u0 = sw16add(v0, v2), u1 = sw16add(v1, v3);
        u0 += dpp<ROR8>(u0); u1 += dpp<ROR8>(u1);
        *(LAS unsigned*)(L + L_PART + w * 128 + (16 * (2 * (pc8 >> 2) + (lane >> 5)) + 4 * (pc8 & 3) + 2 * (g & 1)) * 2) = pkbf(u0, u1);
        float lw = f0 * __uint_as_float(hd[0].y) + f1 * __uint_as_float(hd[1].y);
        lw += dpp<ROR8>(lw); lw = sw16add(lw, lw); lw = sw32add(lw, lw);
        *(LAS float*)(L + L_ML + w * 4 + (lane & 1) * 32) = (lane & 1) ? lw : mw;
    }
    BAR();
    if (!isA) { STOUT(); return; }
    const int un = ((43 * (h2 + 1)) >> 3) - ((43 * h2) >> 3);
    const int sl = lane & 31, hoff = WS_HP + (h2 * 8 + (lane >> 5)) * 512 + (sl < 24 ? sl : 0) * 16;
    v4u gh[4];
#pragma unroll
    for (int k = 0; k < 4; ++k) gh[k] = __builtin_amdgcn_raw_buffer_load_b128(rs, hoff + k * 1024, 0, SC1);
    {
        const v4u a0 = *(LAS v4u*)(L + L_PART + (rho & 7) * 128 + g * 16), a1 = *(LAS v4u*)(L + L_PART + (rho & 7) * 128 + 64 + g * 16);
        const f32x4 ma = *(LAS f32x4*)(L + L_ML + (g & 1) * 16), mb = *(LAS f32x4*)(L + L_ML + 16 - (g & 1) * 16), la = *(LAS f32x4*)(L + L_ML + 32 + (g & 1) * 16);
        f32x4 c0 = mfma(a0, bv[0], f32x4{0.f, 0.f, 0.f, 0.f}), c1 = mfma(a0, bv[2], f32x4{0.f, 0.f, 0.f, 0.f});
        c0 = mfma(a1, bv[1], c0); c1 = mfma(a1, bv[3], c1);
        const float mx = vmax3(vmax3(ma.x, ma.y, ma.z), vmax3(ma.w, mb.x, mb.y), vmax(mb.z, mb.w));
        const f32x4 f = {ex2(ma.x - mx), ex2(ma.y - mx), ex2(ma.z - mx), ex2(ma.w - mx)};
        float den = (f.x * la.x + f.y * la.y) + (f.z * la.z + f.w * la.w);
        den = __builtin_amdgcn_rcpf(sw16add(den, den));
        float r0 = (f.x * c0.x + f.y * c0.y) + (f.z * c0.z + f.w * c0.w), r1 = (f.x * c1.x + f.y * c1.y) + (f.z * c1.z + f.w * c1.w);
        r0 = sw16add(r0, r0) * den; r1 = sw16add(r1, r1) * den;
        const v4u o = {__float_as_uint(r0), tag, __float_as_uint(r1), tag};
        if (lane < 16) __builtin_amdgcn_raw_buffer_store_b128(o, rs, WS_CP + (h * 8 + j) * 1024 + (16 * wa + rho) * 16, 0, SC1);
    }
    ST(14);
    SB();
    float yp;
    {
        for (unsigned spin = 0;; ++spin) {
            unsigned bad = 0;
#pragma unroll
            for (int k = 0; k < 4; ++k) bad |= (gh[k].y ^ tag) | (gh[k].w ^ tag);
            if (__builtin_amdgcn_ballot_w64(bad != 0) == 0 || spin > SPIN_CAP) break;
            CBAR();
#pragma unroll
            for (int k = 0; k < 4; ++k) gh[k] = __builtin_amdgcn_raw_buffer_load_b128(rs, hoff + k * 1024, 0, SC1);
        }
        const float h0 = (__uint_as_float(gh[0].x) + __uint_as_float(gh[1].x)) + (__uint_as_float(gh[2].x) + __uint_as_float(gh[3].x));
        const float h1 = (__uint_as_float(gh[0].z) + __uint_as_float(gh[1].z)) + (__uint_as_float(gh[2].z) + __uint_as_float(gh[3].z));
        float hv = silu(sw32add(h0, h1));
        hv = sl < 4 * un ? hv : 0.f;
        *(LAS u16*)(L + L_HV + wa * 128 + (2 * sl + (lane >> 5)) * 2) = (u16)pkbf(hv, hv);
        asm volatile("s_waitcnt lgkmcnt(0)" ::: "memory");
        f32x4 c = mfma(*(LAS v4u*)(L + L_HV + wa * 128 + g * 16), by[0], f32x4{0.f, 0.f, 0.f, 0.f});
        c = mfma(*(LAS v4u*)(L + L_HV + wa * 128 + 64 + g * 16), by[1], c);
        yp = c.x;
    }
    const unsigned xo = *(const GAS unsigned*)(x + 64 * j2 + 2 * h2);
    ST(15);
    SB();
    { const unsigned np = gather8(rs, WS_CP + (h2 * 8 + g) * 1024 + (16 * wa + rho) * 16, tag, L + L_QV + (32 * wa + 16 * (lane >> 5) + rho) * 2); STV(27, np); }
    ST(16);
    BAR();
    {
        f32x4 c = {0.f, 0.f, 0.f, 0.f};
#pragma unroll
        for (int s = 0; s < 4; ++s) c = mfma(*(LAS v4u*)(L + L_QV + s * 64 + g * 16), bo[s], c);
        const v4u o = {__float_as_uint(c.x), __float_as_uint(yp), tag, ~tag};
        if (lane < 16) __builtin_amdgcn_raw_buffer_store_b128(o, rs, WS_FP + (j2 * 32 + h2) * 1024 + (16 * wa + rho) * 16, 0, SC1);
    }
    ST(17);
    if (wa != 0) { STOUT(); return; }
    {
        __builtin_amdgcn_s_sleep(SLP5);
        v4u gf;
        for (unsigned spin = 0;; ++spin) {
            CBAR();
            gf = __builtin_amdgcn_raw_buffer_load_b128(rs, WS_FP + (j2 * 32 + (lane >> 1)) * 1024 + (2 * h2 + (lane & 1)) * 16, 0, SC1);
            if (__builtin_amdgcn_ballot_w64(((gf.z ^ tag) | (gf.w ^ ~tag)) != 0) == 0 || spin > SPIN_CAP) { STV(28, spin); break; }
        }
        float v = sw32add(__uint_as_float(gf.x), __uint_as_float(gf.y));
        v = sw16add(v, v); v += dpp<ROR8>(v); v += dpp<ROR4>(v); v += dpp<ROR2>(v);
        auto r = __builtin_amdgcn_permlane32_swap(__float_as_uint(v), __float_as_uint(v), false, false);
        const float xe = (lane & 1) ? bfhi(xo) : bflo(xo);
        const float res = xe + __uint_as_float(r[0]) * silu(__uint_as_float(r[1]));
        const float other = dpp<QX1>(res);
        if (lane == 0) {
            out[32 * j2 + h2] = pkbf(res, other);
            if (b == 0) __hip_atomic_store((GAS unsigned*)(ws + WS_E), tag, __ATOMIC_RELAXED, __HIP_MEMORY_SCOPE_AGENT);
        }
        ST(18);
    }
    STOUT();
}

extern "C" void kernel_launch(void* const* d_in, const int* in_sizes, int n_in, void* d_out, int out_size, void* d_ws, size_t ws_size, hipStream_t stream) {
    (void)in_sizes; (void)n_in; (void)out_size; (void)ws_size;
    (void)hipFuncSetAttribute((const void*)mla_kernel, hipFuncAttributeMaxDynamicSharedMemorySize, L_END);
    mla_kernel<<<256, 512, L_END, stream>>>((const u16*)d_in[0], (const char*)d_in[1], (const char*)d_in[2], (const char*)d_in[3], (const char*)d_in[4],
                                            (const char*)d_in[5], (const char*)d_in[7], (const char*)d_in[8], (unsigned*)d_out, (char*)d_ws);
}
```

```cpp
#define PROBE 0
#define PROBEW 1
#include <hip/hip_runtime.h>
#include <hip/hip_bf16.h>

#define STAMPS 0
#define DMA_NT 0
#define W1 7
#define W2 11
#define SLP1 4
#define SLP2 6
#define SLP3 12
#define SLP5 6
#define NOSTREAM 0
#define PSLP 0
#define CODE_KB 12

#define GAS __attribute__((address_space(1)))
#define LAS __attribute__((address_space(3)))
#define DI __device__ __forceinline__
typedef unsigned v4u __attribute__((ext_vector_type(4)));
typedef unsigned v2u __attribute__((ext_vector_type(2)));
typedef float f32x4 __attribute__((ext_vector_type(4)));
typedef float f32x2 __attribute__((ext_vector_type(2)));
typedef short s16x4 __attribute__((ext_vector_type(4)));
typedef short s16x8 __attribute__((ext_vector_type(8)));
typedef __bf16 bf16x8 __attribute__((ext_vector_type(8)));
typedef __bf16 bf16x2 __attribute__((ext_vector_type(2)));
typedef unsigned long long u64;
typedef unsigned short u16;

namespace {
constexpr int F = 1376;
constexpr int SC1 = 16;
constexpr int RSRC_W3 = 0x00020000;
constexpr unsigned SPIN_CAP = 1u << 16;

constexpr int WS_E = 0;
constexpr int WS_QP = 4096;
constexpr int WS_QA = WS_QP + 256 * 1024;
constexpr int WS_HP = WS_QA + 256 * 256;
constexpr int WS_CP = WS_HP + 256 * 512;
constexpr int WS_FP = WS_CP + 256 * 1024;
constexpr int OSTR = 18 * 1024 + 256;
constexpr int WS_OP = WS_FP + 256 * 1024;
constexpr int WS_END = WS_OP + 256 * OSTR;
constexpr int WS_DBG = 8 << 20;

constexpr int L_RING = 0;
constexpr int L_WV = 0;
constexpr int L_WO = 16384;
constexpr int L_W2 = 32768;
constexpr int L_W1 = 40960;
constexpr int L_Q = 131072;
constexpr int L_PX = L_Q + 16384;
constexpr int L_MX = L_PX + 2048;
constexpr int L_VF = L_MX + 256;
constexpr int L_QV = L_VF + 64;
constexpr int L_PART = L_QV + 256;
constexpr int L_ML = L_PART + 1024;
constexpr int L_HV = L_ML + 64;
constexpr int L_JUNK = L_HV + 512;
constexpr int L_END = L_JUNK + 1024;
static_assert(L_END <= 160 * 1024 && L_JUNK % 16 == 0, "lds");

DI float bflo(unsigned u) { return __uint_as_float(u << 16); }
DI float bfhi(unsigned u) { return __uint_as_float(u & 0xffff0000u); }
DI unsigned pkbf(float a, float b) { f32x2 f = {a, b}; return __builtin_bit_cast(unsigned, __builtin_convertvector(f, bf16x2)); }
template <int C> DI float dpp(float v) { return __builtin_amdgcn_update_dpp(0.f, v, C, 0xf, 0xf, true); }
template <int C> DI unsigned dppu(unsigned v) { return (unsigned)__builtin_amdgcn_update_dpp(0, (int)v, C, 0xf, 0xf, true); }
constexpr int ROR1 = 0x121, ROR2 = 0x122, ROR4 = 0x124, ROR8 = 0x128, QX1 = 0xB1, QX2 = 0x4E, HMIR = 0x141;
DI float sw32add(float a, float b) { auto r = __builtin_amdgcn_permlane32_swap(__float_as_uint(a), __float_as_uint(b), false, false); return __uint_as_float(r[0]) + __uint_as_float(r[1]); }
DI float sw16add(float a, float b) { auto r = __builtin_amdgcn_permlane16_swap(__float_as_uint(a), __float_as_uint(b), false, false); return __uint_as_float(r[0]) + __uint_as_float(r[1]); }
DI float vmax(float a, float b) { float r; asm("v_max_f32 %0, %1, %2" : "=v"(r) : "v"(a), "v"(b)); return r; }
DI float vmax3(float a, float b, float c) { float r; asm("v_max3_f32 %0, %1, %2, %3" : "=v"(r) : "v"(a), "v"(b), "v"(c)); return r; }
DI float sw32max(float a) { auto r = __builtin_amdgcn_permlane32_swap(__float_as_uint(a), __float_as_uint(a), false, false); return fmaxf(__uint_as_float(r[0]), __uint_as_float(r[1])); }
DI float sw16max(float a) { auto r = __builtin_amdgcn_permlane16_swap(__float_as_uint(a), __float_as_uint(a), false, false); return fmaxf(__uint_as_float(r[0]), __uint_as_float(r[1])); }
DI float wave_sum(float v) { v += dpp<ROR8>(v); v += dpp<ROR4>(v); v += dpp<ROR2>(v); v += dpp<ROR1>(v); v = sw16add(v, v); return sw32add(v, v); }
DI float ex2(float v) { return __builtin_amdgcn_exp2f(v); }
DI float silu(float v) { return v * __builtin_amdgcn_rcpf(1.f + ex2(v * -1.4426950408889634f)); }
DI f32x4 mfma(v4u a, v4u b, f32x4 c) { return __builtin_amdgcn_mfma_f32_16x16x32_bf16(__builtin_bit_cast(bf16x8, a), __builtin_bit_cast(bf16x8, b), c, 0, 0, 0); }
DI v4u trrd(LAS char* p0, LAS char* p1) {
    const s16x4 lo = __builtin_amdgcn_ds_read_tr16_b64_v4i16((LAS s16x4*)p0), hi = __builtin_amdgcn_ds_read_tr16_b64_v4i16((LAS s16x4*)p1);
    return __builtin_bit_cast(v4u, __builtin_shufflevector(lo, hi, 0, 1, 2, 3, 4, 5, 6, 7));
}
#define BAR() do { asm volatile("s_waitcnt lgkmcnt(0)" ::: "memory"); __builtin_amdgcn_s_barrier(); asm volatile("" ::: "memory"); } while (0)
#define VMW(n) asm volatile("s_waitcnt vmcnt(%0)" :: "i"(n) : "memory")
#define CBAR() asm volatile("" ::: "memory")
#define SB() __builtin_amdgcn_sched_barrier(0)
#define ST(k) do { if ((k) == 0) { t0_ = __builtin_amdgcn_s_memrealtime(); tp_ = 0; } if ((k) == PROBE && (PROBEW == (w >= 4))) { SB(); tp_ = __builtin_amdgcn_s_memrealtime(); asm volatile("s_waitcnt lgkmcnt(0)" ::: "memory"); SB(); } } while (0)
#define STOUT() do { if (tp_) { const u64 te_ = t0_ + 4000 + 4 * (tp_ - t0_); for (int g_ = 0; g_ < 1000000 && __builtin_amdgcn_s_memrealtime() < te_; ++g_) __builtin_amdgcn_s_sleep(1); } } while (0)
#define STV(k, v) (void)(v)
#define NTS ""
DI void dma16(unsigned m0v, unsigned voff, const char* base) {
    asm volatile("s_mov_b32 m0, %0\n\ts_nop 0\n\tglobal_load_lds_dwordx4 %1, %2" :: "s"(m0v), "v"(voff), "s"(base) : "memory");
}
template <int W, int R0, int R1> DI void issue_rows(const char* gbase, unsigned lds0, unsigned vbase) {
#pragma unroll
    for (int r = R0; r < R1; ++r) {
        const int i = r & 15, n = r >> 4;
        const unsigned voff = vbase ^ ((i << 4) | ((i >> 2) << 12) | (n << 16));
        if (r == R0 || (i & 3) == 0) {
            const unsigned m0v = lds0 + ((n & 1) << 16) + ((i >> 2) << 12);
            asm volatile("s_mov_b32 m0, %0\n\ts_waitcnt vmcnt(%3)\n\tglobal_load_lds_dwordx4 %1, %2 offset:%4" NTS
                         :: "s"(m0v), "v"(voff), "s"(gbase), "i"(W - 1), "i"((i & 3) * 1024) : "memory");
        } else {
            asm volatile("s_waitcnt vmcnt(%2)\n\tglobal_load_lds_dwordx4 %0, %1 offset:%3" NTS
                         :: "v"(voff), "s"(gbase), "i"(W - 1), "i"((i & 3) * 1024) : "memory");
        }
    }
}
DI unsigned gather8(__amdgpu_buffer_rsrc_t rs, int off, unsigned tag, LAS char* dst) {
    float c0, c1; unsigned spin;
    for (spin = 0;; ++spin) {
        CBAR();
        const v4u g0 = __builtin_amdgcn_raw_buffer_load_b128(rs, off, 0, SC1), g1 = __builtin_amdgcn_raw_buffer_load_b128(rs, off, 4096, SC1);
        const unsigned bad = ((g0.y ^ tag) | (g0.w ^ tag)) | ((g1.y ^ tag) | (g1.w ^ tag));
        c0 = __uint_as_float(g0.x) + __uint_as_float(g1.x); c1 = __uint_as_float(g0.z) + __uint_as_float(g1.z);
        if (__builtin_amdgcn_ballot_w64(bad != 0) == 0 || spin > SPIN_CAP) break;
        if (PSLP) __builtin_amdgcn_s_sleep(PSLP);
    }
    float cx = sw32add(c0, c1);
    cx = sw16add(cx, cx);
    *(LAS u16*)dst = (u16)pkbf(cx, cx);
    return spin;
}
}

__global__ void __launch_bounds__(512, 2)
mla_kernel(const u16* __restrict__ x, const char* __restrict__ ckv, const char* __restrict__ wq, const char* __restrict__ wuk,
           const char* __restrict__ wuv, const char* __restrict__ wo, const char* __restrict__ w1, const char* __restrict__ w2,
           unsigned* __restrict__ out, char* ws) {
    extern __shared__ __attribute__((aligned(16))) char smem_[];
    LAS char* L = (LAS char*)smem_;
    u64 t0_, tp_;
    const char* pc;
    asm volatile("s_getpc_b64 %0" : "=s"(pc));
    pc = (const char*)((size_t)pc & ~(size_t)255);
    const int lane = threadIdx.x & 63, w = __builtin_amdgcn_readfirstlane(threadIdx.x >> 6), wa = w & 3;
    const bool isA = w >= 4;
    asm volatile("" :: "s"(x), "s"(ckv), "s"(wq), "s"(wuk), "s"(wuv), "s"(wo), "s"(w1), "s"(w2), "s"(out), "s"(ws));
    ST(0);
    const int b = blockIdx.x, xcd = b & 7, i8 = b >> 3, a = i8 & 1, ip = i8 >> 1;
    const int p = xcd * 16 + ip;
    const int hh = ip, h = 16 * a + hh, j = xcd;
    const int h2 = 4 * xcd + (i8 >> 3), j2 = i8 & 7;
    const int g = lane >> 4, rho = lane & 15;
    const unsigned lbase = (unsigned)(size_t)L;

    unsigned e0; v4u xv, xf0, xf1;
    {
        const unsigned o1 = lane * 16, o2 = 128 * j + 16 * g;
        asm volatile("global_load_dword %0, %4, %5 sc1\n\tglobal_load_dwordx4 %1, %6, %7\n\tglobal_load_dwordx4 %2, %8, %7\n\tglobal_load_dwordx4 %3, %8, %7 offset:64"
                     : "=&v"(e0), "=&v"(xv), "=&v"(xf0), "=&v"(xf1) : "v"(0), "s"(ws), "v"(o1), "s"(x), "v"(o2) : "memory");
    }
    const size_t wrow = ((size_t)(64 * j) * 4096 + 128 * h + 32 * wa) * 2;
    const unsigned wvoff = (lane >> 2) * 8192 + (((lane & 3) ^ ((lane >> 4) & 2)) << 4);
    const char* gbase = ckv + ((size_t)p * 256 + wa * 16) * 1024;
    {
        const char* sb = isA ? gbase : wq + wrow;
        const unsigned vb = isA ? (unsigned)lane << 4 : wvoff, xm = isA ? 16u : 0u, gs = isA ? 1024u : 131072u;
        const unsigned lb = lbase + (isA ? L_RING + wa * 16384 : L_Q + wa * 4096);
#pragma unroll
        for (int i = 0; i < 4; ++i) dma16(lb + i * 1024, (vb ^ (i * xm)) + i * gs, sb);
        dma16(lbase + L_JUNK, (unsigned)lane << 4, pc + ((w + 8 * (i8 & 1)) % CODE_KB) * 1024);
    }
    if (threadIdx.x < 8) *(volatile LAS unsigned*)(L + L_VF + 4 * lane) = 0u;
    BAR();
    const __amdgpu_buffer_rsrc_t rs = __builtin_amdgcn_make_buffer_rsrc(ws, 0, WS_END, RSRC_W3);
    const v4u ones = {0x3f803f80u, 0x3f803f80u, 0x3f803f80u, 0x3f803f80u};
    const int q4 = rho >> 2, p4 = lane & 3;
    const unsigned tb1 = wa * 4096 + (8 * g + q4) * 64 + (((p4 >> 1) ^ (2 * (g & 1))) << 4) + (p4 & 1) * 8;
    unsigned tag; float mean, rstd;
#define LN_STATS(NY) do { \
        asm volatile("s_waitcnt vmcnt(%4)" : "+v"(e0), "+v"(xv), "+v"(xf0), "+v"(xf1) : "i"(NY) : "memory"); \
        const float xf[8] = {bflo(xv.x), bfhi(xv.x), bflo(xv.y), bfhi(xv.y), bflo(xv.z), bfhi(xv.z), bflo(xv.w), bfhi(xv.w)}; \
        mean = wave_sum(((xf[0] + xf[1]) + (xf[2] + xf[3])) + ((xf[4] + xf[5]) + (xf[6] + xf[7]))) * (1.f / 512); \
        float vs = 0.f; \
        _Pragma("unroll") for (int i = 0; i < 8; ++i) { const float dd = xf[i] - mean; vs += dd * dd; } \
        rstd = __builtin_amdgcn_rsqf(wave_sum(vs) * (1.f / 512) + 1e-5f); \
        tag = __builtin_amdgcn_readfirstlane(e0) + 1u; \
        xf0 = rho == 0 ? xf0 : ones; xf1 = rho == 0 ? xf1 : ones; \
    } while (0)
    LN_STATS(5);

    if (!isA) {
        v4u wk[4];
        {
            const char* kb = wuk + ((size_t)(64 * j + 16 * wa) * 4096 + 128 * h) * 2;
            const unsigned ko = rho * 8192 + g * 16;
            asm volatile("global_load_dwordx4 %0, %4, %5\n\tglobal_load_dwordx4 %1, %4, %5 offset:64\n\tglobal_load_dwordx4 %2, %4, %5 offset:128\n\tglobal_load_dwordx4 %3, %4, %5 offset:192"
                         : "=&v"(wk[0]), "=&v"(wk[1]), "=&v"(wk[2]), "=&v"(wk[3]) : "v"(ko), "s"(kb) : "memory");
        }
        ST(1);
        VMW(5);
        {
            f32x4 c[2];
#pragma unroll
            for (int e = 0; e < 2; ++e) {
                LAS char* t = L + L_Q + (tb1 ^ (e * 32));
                c[e] = mfma(xf0, trrd(t, t + 256), f32x4{0.f, 0.f, 0.f, 0.f});
                c[e] = mfma(xf1, trrd(t + 2048, t + 2304), c[e]);
            }
            const v4u o = {__float_as_uint(rstd * (c[0].x - mean * c[0].y)), tag, __float_as_uint(rstd * (c[1].x - mean * c[1].y)), tag};
            asm volatile("s_waitcnt lgkmcnt(0)" ::: "memory");
            if (lane < 16) __builtin_amdgcn_raw_buffer_store_b128(o, rs, WS_QP + (h * 8 + j) * 1024 + (16 * wa + rho) * 16, 0, SC1);
        }
        ST(2);
        __builtin_amdgcn_s_sleep(SLP1);
        { const unsigned np = gather8(rs, WS_QP + (h * 8 + g) * 1024 + (16 * wa + rho) * 16, tag, L + L_QV + (32 * wa + 16 * (lane >> 5) + rho) * 2); STV(24, np); }
        ST(3);
        if (lane == 0) __hip_atomic_fetch_add((LAS unsigned*)(L + L_VF + 16), 1u, __ATOMIC_RELAXED, __HIP_MEMORY_SCOPE_WORKGROUP);
        {
            v4u qb[4];
            for (unsigned spin = 0;; ++spin) {
                CBAR();
                const unsigned cnt = *(volatile LAS unsigned*)(L + L_VF + 16);
                CBAR();
#pragma unroll
                for (int s = 0; s < 4; ++s) qb[s] = *(LAS v4u*)(L + L_QV + s * 64 + g * 16);
                CBAR();
                if (__builtin_amdgcn_readfirstlane(cnt) >= 4u || spin > SPIN_CAP * 16) break;
            }
            asm volatile("" : "+v"(wk[0]), "+v"(wk[1]), "+v"(wk[2]), "+v"(wk[3]));
            f32x4 cv = {0.f, 0.f, 0.f, 0.f};
#pragma unroll
            for (int s = 0; s < 4; ++s) cv = mfma(wk[s], qb[s], cv);
            cv *= 0.08838834764831845f * 1.4426950408889634f;
            const v4u o = {pkbf(cv.x, cv.y), pkbf(cv.z, cv.w), tag, ~tag};
            if (rho == 0) __builtin_amdgcn_raw_buffer_store_b128(o, rs, WS_QA + (a * 128 + hh * 8 + j) * 256 + (wa * 4 + g) * 16, 0, SC1);
        }
        ST(4);
        const int z = 8 * (g & 1) + 2 * q4 + (g >> 1);
        unsigned ta[8];
#pragma unroll
        for (int t = 0; t < 8; ++t) { ta[t] = lbase + z * 1024 + 256 * wa + 16 * ((2 * t + (p4 >> 1)) ^ z) + 8 * (p4 & 1); asm volatile("" : "+v"(ta[t])); }
        f32x4 acc[8], lacc = {0.f, 0.f, 0.f, 0.f};
#pragma unroll
        for (int t = 0; t < 8; ++t) { acc[t] = f32x4{0.f, 0.f, 0.f, 0.f}; asm volatile("" : "+v"(acc[t])); }
        __builtin_amdgcn_s_sleep(SLP2);
        {
            const int go = WS_QA + (a * 128 + wa * 32) * 256 + lane * 16;
            LAS char* qd = L + L_Q + ((2 * g + (p4 >> 1)) * 64 + q4 * 16 + 4 * wa) * 16 + (p4 & 1) * 8;
            for (unsigned spin = 0;; ++spin) {
                CBAR();
                v4u gq[8];
#pragma unroll
                for (int k = 0; k < 8; ++k) gq[k] = __builtin_amdgcn_raw_buffer_load_b128(rs, go + (k & 3) * 1024, (k >> 2) * 4096, SC1);
                unsigned bad = 0;
#pragma unroll
                for (int k = 0; k < 8; ++k) {
                    bad |= (gq[k].z ^ tag) | (gq[k].w ^ ~tag);
                    *(LAS v2u*)(qd + (k & 1) * 8192 + (k >> 1) * 16) = v2u{gq[k].x, gq[k].y};
                }
                if (__builtin_amdgcn_ballot_w64(bad != 0) == 0 || spin > SPIN_CAP) { STV(25, spin); break; }
                if (PSLP) __builtin_amdgcn_s_sleep(PSLP);
            }
        }
        ST(5);
        BAR();
        float mref = -1e30f;
#pragma nounroll
        for (int n = 0; n < 4; ++n) {
            v4u tv[2][8];
#pragma unroll
            for (int ks = 0; ks < 2; ++ks)
#pragma unroll
                for (int t = 0; t < 8; ++t) tv[ks][t] = trrd((LAS char*)(size_t)ta[t] + ks * 32768, (LAS char*)(size_t)ta[t] + ks * 32768 + 16384);
#pragma unroll
            for (int t = 0; t < 8; ++t) ta[t] ^= 65536u;
            BAR();
            const unsigned vf = *(volatile LAS unsigned*)(L + L_VF + 4 * n);
            v4u pf0 = *(LAS v4u*)(L + L_PX + lane * 16), pf1 = *(LAS v4u*)(L + L_PX + 1024 + lane * 16);
            if (__builtin_amdgcn_readfirstlane(vf) != 0) {
                BAR();
                const f32x4 m4 = *(LAS f32x4*)(L + L_MX + rho * 16);
                const float mn = fmaxf(fmaxf(fmaxf(m4.x, m4.y), fmaxf(m4.z, m4.w)), mref);
                const float al = ex2(mref - mn);
                mref = mn;
#pragma unroll
                for (int t = 0; t < 8; ++t) acc[t] *= al;
                lacc *= al;
                BAR();
                pf0 = *(LAS v4u*)(L + L_PX + lane * 16); pf1 = *(LAS v4u*)(L + L_PX + 1024 + lane * 16);
            }
#pragma unroll
            for (int t = 0; t < 8; ++t) acc[t] = mfma(tv[0][t], pf0, acc[t]);
            lacc = mfma(ones, pf0, lacc);
            lacc = mfma(ones, pf1, lacc);
#pragma unroll
            for (int t = 0; t < 8; ++t) acc[t] = mfma(tv[1][t], pf1, acc[t]);
            if (n == 0) ST(6); else if (n == 1) ST(7); else if (n == 2) ST(8); else ST(9);
            if (n < 3) BAR();
        }
        const int ob = WS_OP + (a * 128 + p) * OSTR;
        v4u o[4];
#pragma unroll
        for (int k = 0; k < 4; ++k) {
            const f32x4 t0 = acc[2 * k], t1 = acc[2 * k + 1];
            o[k] = v4u{pkbf(t0.x, t0.y), pkbf(t0.z, t0.w), pkbf(t1.x, t1.y), pkbf(t1.z, t1.w)};
            __builtin_amdgcn_raw_buffer_store_b128(o[k], rs, wa * 4096 + rho * 64 + g * 16 + k * 1024, ob, SC1);
        }
        SB();
        {
            const unsigned c0 = (o[0].x + o[0].y + o[0].z) + (o[0].w + o[1].x + o[1].y) + (o[1].z + o[1].w);
            const unsigned c1 = (o[2].x + o[2].y + o[2].z) + (o[2].w + o[3].x + o[3].y) + (o[3].z + o[3].w);
            auto r1 = __builtin_amdgcn_permlane32_swap(c0, c1, false, false); unsigned s1 = r1[0] + r1[1];
            auto r3 = __builtin_amdgcn_permlane16_swap(s1, s1, false, false); s1 = r3[0] + r3[1];
            const unsigned mb = __float_as_uint(mref), lb = __float_as_uint(lacc.x);
            const v4u hd = {mb, lb, s1 + mb + lb, tag};
            if ((lane & 16) == 0) __builtin_amdgcn_raw_buffer_store_b128(hd, rs, 16384 + (2 * wa + (lane >> 5)) * 256 + rho * 16, ob, SC1);
        }
        ST(10);
        __builtin_amdgcn_s_sleep(SLP3);
    } else {
        const unsigned lds0 = lbase + L_RING + wa * 16384;
        const unsigned vbase = lane << 4;
        ST(1);
        issue_rows<W1, 4, 32>(gbase, lds0, vbase);
        const int kap = 8 * (q4 & 1) + 2 * (rho & 3) + (rho >> 3);
        unsigned sa[4];
#pragma unroll
        for (int s = 0; s < 4; ++s) { sa[s] = lbase + (16 * wa + kap) * 1024 + 16 * ((8 * (s >> 1) + 2 * g + (s & 1)) ^ kap); asm volatile("" : "+v"(sa[s])); }
        LAS v2u* pxw = (LAS v2u*)(L + L_PX + (wa >> 1) * 1024 + lane * 16 + (wa & 1) * 8);
        __builtin_amdgcn_s_setprio(1);
        ST(2);
        BAR();
        ST(3);
        v4u qf[16];
#pragma unroll
        for (int s = 0; s < 16; ++s) qf[s] = *(LAS v4u*)(L + L_Q + s * 1024 + lane * 16);
        float mref = -1e30f;
        const int U0 = (43 * h) >> 3, un = ((43 * (h + 1)) >> 3) - U0;
#pragma nounroll
        for (int n = 0;; ++n) {
            f32x4 sv = {0.f, 0.f, 0.f, 0.f};
            {
                v4u af[16];
#pragma unroll
                for (int s = 0; s < 16; ++s) af[s] = *(LAS v4u*)((LAS char*)(size_t)sa[s & 3] + (s >> 2) * 256);
                SB();
#pragma unroll
                for (int s = 0; s < 16; ++s) sv = mfma(af[s], qf[s], sv);
            }
            f32x4 d = sv - mref;
            *pxw = v2u{pkbf(ex2(d.x), ex2(d.y)), pkbf(ex2(d.z), ex2(d.w))};
            if (!(vmax(vmax3(d.x, d.y, d.z), d.w) <= 10.f)) *(volatile LAS unsigned*)(L + L_VF + 4 * n) = 1u;
            BAR();
            if (__builtin_amdgcn_readfirstlane(*(volatile LAS unsigned*)(L + L_VF + 4 * n)) != 0) {
                float tm = fmaxf(fmaxf(sv.x, sv.y), fmaxf(sv.z, sv.w));
                tm = sw32max(sw16max(tm));
                if (lane < 16) *(LAS float*)(L + L_MX + lane * 16 + wa * 4) = tm;
                BAR();
                const f32x4 m4 = *(LAS f32x4*)(L + L_MX + rho * 16);
                mref = fmaxf(fmaxf(fmaxf(m4.x, m4.y), fmaxf(m4.z, m4.w)), mref);
                d = sv - mref;
                *pxw = v2u{pkbf(ex2(d.x), ex2(d.y)), pkbf(ex2(d.z), ex2(d.w))};
                BAR();
            }
            if (n == 0) ST(4); else if (n == 1) ST(6); else if (n == 2) ST(8); else ST(10);
            if (n == 3) break;
#pragma unroll
            for (int s = 0; s < 4; ++s) sa[s] ^= 65536u;
            if (n == 0) { issue_rows<W2, 32, 32 + W2>(gbase, lds0, vbase); VMW(W2); }
            else if (n == 1) { issue_rows<W2, 32 + W2, 64>(gbase, lds0, vbase); }
            else {
#pragma unroll
                for (int i = 0; i < 2; ++i) {
                    const int uc = 2 * wa + (lane & 1);
                    dma16(lbase + L_W1 + wa * 2048 + i * 1024, (32 * i + (lane >> 1)) * (F * 2) + (uc < un ? uc : 0) * 16, w1 + (size_t)(64 * j) * (F * 2) + U0 * 16);
                }
#pragma unroll
                for (int i = 0; i < 4; ++i) dma16(lbase + L_WV + wa * 4096 + i * 1024, wvoff + i * 131072, wuv + wrow);
                const int r8 = lane >> 3;
#pragma unroll
                for (int i = 0; i < 4; ++i)
                    dma16(lbase + L_WO + (4 * wa + i) * 1024, (8 * i + r8) * 1024 + (((lane & 7) ^ ((r8 & 2) + 4 * (i & 1))) << 4), wo + ((size_t)(h2 * 128 + 32 * wa) * 512 + 64 * j2) * 2);
#pragma unroll
                for (int i = 0; i < 2; ++i) {
                    const int k = 2 * wa + i;
                    const int V0 = (43 * h2) >> 3, vn = ((43 * (h2 + 1)) >> 3) - V0;
                    dma16(lbase + L_W2 + k * 1024, r8 * 1024 + (((lane & 7) ^ ((r8 & 2) + 4 * (i & 1))) << 4), w2 + (size_t)(8 * (V0 + (k < vn ? k : 0))) * 1024 + 128 * j2);
                }
                VMW(12);
            }
            if (n == 0) ST(5); else if (n == 1) ST(7); else ST(9);
            BAR();
        }
        __builtin_amdgcn_s_setprio(0);
        VMW(10);
        {
            LAS char* t = L + L_W1 + wa * 2048 + (8 * g + q4) * 32 + p4 * 8;
            f32x4 c = mfma(xf0, trrd(t, t + 128), f32x4{0.f, 0.f, 0.f, 0.f});
            c = mfma(xf1, trrd(t + 1024, t + 1152), c);
            if (lane < 16) __builtin_amdgcn_raw_buffer_store_b64(v2u{__float_as_uint(rstd * (c.x - mean * c.y)), tag}, rs, WS_HP + (h * 8 + j) * 512 + wa * 128 + rho * 8, 0, SC1);
        }
        VMW(1);
        ST(11);
    }
    BAR();
    ST(12);
    v4u bv[4], bo[4], by[2];
    {
        const int msg = lane >> 3, pc8 = lane & 7;
        const int base = WS_OP + (a * 128 + w * 16 + msg) * OSTR;
        const int po = base + (j * 2 + (pc8 >> 2)) * 1024 + hh * 64 + (pc8 & 3) * 16, ho = base + 16384 + j * 256 + hh * 16;
        v4u pl[2], hd[2];
#pragma unroll
        for (int k = 0; k < 2; ++k) {
            pl[k] = __builtin_amdgcn_raw_buffer_load_b128(rs, po, k * 8 * OSTR, SC1);
            hd[k] = __builtin_amdgcn_raw_buffer_load_b128(rs, ho, k * 8 * OSTR, SC1);
        }
        SB();
#pragma unroll
        for (int e = 0; e < 2; ++e) { LAS char* t = L + L_WV + (tb1 ^ (e * 32)); bv[2 * e] = trrd(t, t + 256); bv[2 * e + 1] = trrd(t + 2048, t + 2304); }
        {
            const unsigned cb = (((2 * wa + (p4 >> 1)) ^ ((q4 & 2) + 4 * (g & 1))) << 4) + (p4 & 1) * 8;
            LAS char* t = L + L_WO + (8 * g + q4) * 128 + cb;
#pragma unroll
            for (int s = 0; s < 4; ++s) bo[s] = trrd(t + s * 4096, t + s * 4096 + 512);
            by[0] = trrd(t + (L_W2 - L_WO), t + (L_W2 - L_WO) + 512);
            LAS char* t2 = L + L_W2 + 4096 + (8 * (g & 1) + q4) * 128 + cb;
            by[1] = trrd(t2, t2 + 512);
        }
        SB();
        for (unsigned spin = 0;; ++spin) {
            bool ok[2];
#pragma unroll
            for (int k = 0; k < 2; ++k) {
                unsigned s1 = (pl[k].x + pl[k].y + pl[k].z) + pl[k].w;
                s1 += dppu<QX1>(s1); s1 += dppu<QX2>(s1); s1 += dppu<HMIR>(s1);
                ok[k] = (s1 + hd[k].x + hd[k].y == hd[k].z) & (hd[k].w == tag);
            }
            if (__builtin_amdgcn_ballot_w64(!(ok[0] & ok[1])) == 0 || spin > SPIN_CAP) { STV(26, spin); break; }
            if (PSLP) __builtin_amdgcn_s_sleep(PSLP);
            CBAR();
#pragma unroll
            for (int k = 0; k < 2; ++k) if (!ok[k]) {
                pl[k] = __builtin_amdgcn_raw_buffer_load_b128(rs, po, k * 8 * OSTR, SC1);
                hd[k] = __builtin_amdgcn_raw_buffer_load_b128(rs, ho, k * 8 * OSTR, SC1);
            }
        }
        ST(13);
        const float m0 = __uint_as_float(hd[0].x), m1 = __uint_as_float(hd[1].x);
        float mw = fmaxf(m0, m1);
        mw = fmaxf(mw, dpp<ROR8>(mw)); mw = sw32max(sw16max(mw));
        const float f0 = ex2(m0 - mw), f1 = ex2(m1 - mw);
        float a8[8];
        a8[0] = f0 * bflo(pl[0].x) + f1 * bflo(pl[1].x); a8[1] = f0 * bfhi(pl[0].x) + f1 * bfhi(pl[1].x);
        a8[2] = f0 * bflo(pl[0].y) + f1 * bflo(pl[1].y); a8[3] = f0 * bfhi(pl[0].y) + f1 * bfhi(pl[1].y);
        a8[4] = f0 * bflo(pl[0].z) + f1 * bflo(pl[1].z); a8[5] = f0 * bfhi(pl[0].z) + f1 * bfhi(pl[1].z);
        a8[6] = f0 * bflo(pl[0].w) + f1 * bflo(pl[1].w); a8[7] = f0 * bfhi(pl[0].w) + f1 * bfhi(pl[1].w);
        const float v0 = sw32add(a8[0], a8[4]), v1 = sw32add(a8[1], a8[5]), v2 = sw32add(a8[2], a8[6]), v3 = sw32add(a8[3], a8[7]);
        float u0 = sw16add(v0, v2), u1 = sw16add(v1, v3);
        u0 += dpp<ROR8>(u0); u1 += dpp<ROR8>(u1);
        *(LAS unsigned*)(L + L_PART + w * 128 + (16 * (2 * (pc8 >> 2) + (lane >> 5)) + 4 * (pc8 & 3) + 2 * (g & 1)) * 2) = pkbf(u0, u1);
        float lw = f0 * __uint_as_float(hd[0].y) + f1 * __uint_as_float(hd[1].y);
        lw += dpp<ROR8>(lw); lw = sw16add(lw, lw); lw = sw32add(lw, lw);
        *(LAS float*)(L + L_ML + w * 4 + (lane & 1) * 32) = (lane & 1) ? lw : mw;
    }
    BAR();
    if (!isA) { STOUT(); return; }
    const int un = ((43 * (h2 + 1)) >> 3) - ((43 * h2) >> 3);
    const int sl = lane & 31, hoff = WS_HP + (h2 * 8 + (lane >> 5)) * 512 + (sl < 24 ? sl : 0) * 16;
    v4u gh[4];
#pragma unroll
    for (int k = 0; k < 4; ++k) gh[k] = __builtin_amdgcn_raw_buffer_load_b128(rs, hoff + k * 1024, 0, SC1);
    {
        const v4u a0 = *(LAS v4u*)(L + L_PART + (rho & 7) * 128 + g * 16), a1 = *(LAS v4u*)(L + L_PART + (rho & 7) * 128 + 64 + g * 16);
        const f32x4 ma = *(LAS f32x4*)(L + L_ML + (g & 1) * 16), mb = *(LAS f32x4*)(L + L_ML + 16 - (g & 1) * 16), la = *(LAS f32x4*)(L + L_ML + 32 + (g & 1) * 16);
        f32x4 c0 = mfma(a0, bv[0], f32x4{0.f, 0.f, 0.f, 0.f}), c1 = mfma(a0, bv[2], f32x4{0.f, 0.f, 0.f, 0.f});
        c0 = mfma(a1, bv[1], c0); c1 = mfma(a1, bv[3], c1);
        const float mx = vmax3(vmax3(ma.x, ma.y, ma.z), vmax3(ma.w, mb.x, mb.y), vmax(mb.z, mb.w));
        const f32x4 f = {ex2(ma.x - mx), ex2(ma.y - mx), ex2(ma.z - mx), ex2(ma.w - mx)};
        float den = (f.x * la.x + f.y * la.y) + (f.z * la.z + f.w * la.w);
        den = __builtin_amdgcn_rcpf(sw16add(den, den));
        float r0 = (f.x * c0.x + f.y * c0.y) + (f.z * c0.z + f.w * c0.w), r1 = (f.x * c1.x + f.y * c1.y) + (f.z * c1.z + f.w * c1.w);
        r0 = sw16add(r0, r0) * den; r1 = sw16add(r1, r1) * den;
        const v4u o = {__float_as_uint(r0), tag, __float_as_uint(r1), tag};
        if (lane < 16) __builtin_amdgcn_raw_buffer_store_b128(o, rs, WS_CP + (h * 8 + j) * 1024 + (16 * wa + rho) * 16, 0, SC1);
    }
    ST(14);
    SB();
    float yp;
    {
        for (unsigned spin = 0;; ++spin) {
            unsigned bad = 0;
#pragma unroll
            for (int k = 0; k < 4; ++k) bad |= (gh[k].y ^ tag) | (gh[k].w ^ tag);
            if (__builtin_amdgcn_ballot_w64(bad != 0) == 0 || spin > SPIN_CAP) break;
            CBAR();
#pragma unroll
            for (int k = 0; k < 4; ++k) gh[k] = __builtin_amdgcn_raw_buffer_load_b128(rs, hoff + k * 1024, 0, SC1);
        }
        const float h0 = (__uint_as_float(gh[0].x) + __uint_as_float(gh[1].x)) + (__uint_as_float(gh[2].x) + __uint_as_float(gh[3].x));
        const float h1 = (__uint_as_float(gh[0].z) + __uint_as_float(gh[1].z)) + (__uint_as_float(gh[2].z) + __uint_as_float(gh[3].z));
        float hv = silu(sw32add(h0, h1));
        hv = sl < 4 * un ? hv : 0.f;
        *(LAS u16*)(L + L_HV + wa * 128 + (2 * sl + (lane >> 5)) * 2) = (u16)pkbf(hv, hv);
        asm volatile("s_waitcnt lgkmcnt(0)" ::: "memory");
        f32x4 c = mfma(*(LAS v4u*)(L + L_HV + wa * 128 + g * 16), by[0], f32x4{0.f, 0.f, 0.f, 0.f});
        c = mfma(*(LAS v4u*)(L + L_HV + wa * 128 + 64 + g * 16), by[1], c);
        yp = c.x;
    }
    const unsigned xo = *(const GAS unsigned*)(x + 64 * j2 + 2 * h2);
    ST(15);
    SB();
    { const unsigned np = gather8(rs, WS_CP + (h2 * 8 + g) * 1024 + (16 * wa + rho) * 16, tag, L + L_QV + (32 * wa + 16 * (lane >> 5) + rho) * 2); STV(27, np); }
    ST(16);
    BAR();
    {
        f32x4 c = {0.f, 0.f, 0.f, 0.f};
#pragma unroll
        for (int s = 0; s < 4; ++s) c = mfma(*(LAS v4u*)(L + L_QV + s * 64 + g * 16), bo[s], c);
        const v4u o = {__float_as_uint(c.x), __float_as_uint(yp), tag, ~tag};
        if (lane < 16) __builtin_amdgcn_raw_buffer_store_b128(o, rs, WS_FP + (j2 * 32 + h2) * 1024 + (16 * wa + rho) * 16, 0, SC1);
    }
    ST(17);
    if (wa != 0) { STOUT(); return; }
    {
        __builtin_amdgcn_s_sleep(SLP5);
        v4u gf;
        for (unsigned spin = 0;; ++spin) {
            CBAR();
            gf = __builtin_amdgcn_raw_buffer_load_b128(rs, WS_FP + (j2 * 32 + (lane >> 1)) * 1024 + (2 * h2 + (lane & 1)) * 16, 0, SC1);
            if (__builtin_amdgcn_ballot_w64(((gf.z ^ tag) | (gf.w ^ ~tag)) != 0) == 0 || spin > SPIN_CAP) { STV(28, spin); break; }
        }
        float v = sw32add(__uint_as_float(gf.x), __uint_as_float(gf.y));
        v = sw16add(v, v); v += dpp<ROR8>(v); v += dpp<ROR4>(v); v += dpp<ROR2>(v);
        auto r = __builtin_amdgcn_permlane32_swap(__float_as_uint(v), __float_as_uint(v), false, false);
        const float xe = (lane & 1) ? bfhi(xo) : bflo(xo);
        const float res = xe + __uint_as_float(r[0]) * silu(__uint_as_float(r[1]));
        const float other = dpp<QX1>(res);
        if (lane == 0) {
            out[32 * j2 + h2] = pkbf(res, other);
            if (b == 0) __hip_atomic_store((GAS unsigned*)(ws + WS_E), tag, __ATOMIC_RELAXED, __HIP_MEMORY_SCOPE_AGENT);
        }
        ST(18);
    }
    STOUT();
}

extern "C" void kernel_launch(void* const* d_in, const int* in_sizes, int n_in, void* d_out, int out_size, void* d_ws, size_t ws_size, hipStream_t stream) {
    (void)in_sizes; (void)n_in; (void)out_size; (void)ws_size;
    (void)hipFuncSetAttribute((const void*)mla_kernel, hipFuncAttributeMaxDynamicSharedMemorySize, L_END);
    mla_kernel<<<256, 512, L_END, stream>>>((const u16*)d_in[0], (const char*)d_in[1], (const char*)d_in[2], (const char*)d_in[3], (const char*)d_in[4],
                                            (const char*)d_in[5], (const char*)d_in[7], (const char*)d_in[8], (unsigned*)d_out, (char*)d_ws);
}
```

```cpp
#define PROBE 0
#define PROBEW 1
#define PROBEK 0
#define PROBEX 1
#include <hip/hip_runtime.h>
#include <hip/hip_bf16.h>

#define STAMPS 0
#define DMA_NT 0
#define W1 7
#define W2 11
#define SLP1 4
#define SLP2 6
#define SLP3 12
#define SLP5 6
#define NOSTREAM 0
#define PSLP 0
#define CODE_KB 12

#define GAS __attribute__((address_space(1)))
#define LAS __attribute__((address_space(3)))
#define DI __device__ __forceinline__
typedef unsigned v4u __attribute__((ext_vector_type(4)));
typedef unsigned v2u __attribute__((ext_vector_type(2)));
typedef float f32x4 __attribute__((ext_vector_type(4)));
typedef float f32x2 __attribute__((ext_vector_type(2)));
typedef short s16x4 __attribute__((ext_vector_type(4)));
typedef short s16x8 __attribute__((ext_vector_type(8)));
typedef __bf16 bf16x8 __attribute__((ext_vector_type(8)));
typedef __bf16 bf16x2 __attribute__((ext_vector_type(2)));
typedef unsigned long long u64;
typedef unsigned short u16;

namespace {
constexpr int F = 1376;
constexpr int SC1 = 16;
constexpr int RSRC_W3 = 0x00020000;
constexpr unsigned SPIN_CAP = 1u << 16;

constexpr int WS_E = 0;
constexpr int WS_QP = 4096;
constexpr int WS_QA = WS_QP + 256 * 1024;
constexpr int WS_HP = WS_QA + 256 * 256;
constexpr int WS_CP = WS_HP + 256 * 512;
constexpr int WS_FP = WS_CP + 256 * 1024;
constexpr int OSTR = 18 * 1024 + 256;
constexpr int WS_OP = WS_FP + 256 * 1024;
constexpr int WS_END = WS_OP + 256 * OSTR;
constexpr int WS_DBG = 8 << 20;

constexpr int L_RING = 0;
constexpr int L_WV = 0;
constexpr int L_WO = 16384;
constexpr int L_W2 = 32768;
constexpr int L_W1 = 40960;
constexpr int L_Q = 131072;
constexpr int L_PX = L_Q + 16384;
constexpr int L_MX = L_PX + 2048;
constexpr int L_VF = L_MX + 256;
constexpr int L_QV = L_VF + 64;
constexpr int L_PART = L_QV + 256;
constexpr int L_ML = L_PART + 1024;
constexpr int L_HV = L_ML + 64;
constexpr int L_JUNK = L_HV + 512;
constexpr int L_END = L_JUNK + 1024;
static_assert(L_END <= 160 * 1024 && L_JUNK % 16 == 0, "lds");

DI float bflo(unsigned u) { return __uint_as_float(u << 16); }
DI float bfhi(unsigned u) { return __uint_as_float(u & 0xffff0000u); }
DI unsigned pkbf(float a, float b) { f32x2 f = {a, b}; return __builtin_bit_cast(unsigned, __builtin_convertvector(f, bf16x2)); }
template <int C> DI float dpp(float v) { return __builtin_amdgcn_update_dpp(0.f, v, C, 0xf, 0xf, true); }
template <int C> DI unsigned dppu(unsigned v) { return (unsigned)__builtin_amdgcn_update_dpp(0, (int)v, C, 0xf, 0xf, true); }
constexpr int ROR1 = 0x121, ROR2 = 0x122, ROR4 = 0x124, ROR8 = 0x128, QX1 = 0xB1, QX2 = 0x4E, HMIR = 0x141;
DI float sw32add(float a, float b) { auto r = __builtin_amdgcn_permlane32_swap(__float_as_uint(a), __float_as_uint(b), false, false); return __uint_as_float(r[0]) + __uint_as_float(r[1]); }
DI float sw16add(float a, float b) { auto r = __builtin_amdgcn_permlane16_swap(__float_as_uint(a), __float_as_uint(b), false, false); return __uint_as_float(r[0]) + __uint_as_float(r[1]); }
DI float vmax(float a, float b) { float r; asm("v_max_f32 %0, %1, %2" : "=v"(r) : "v"(a), "v"(b)); return r; }
DI float vmax3(float a, float b, float c) { float r; asm("v_max3_f32 %0, %1, %2, %3" : "=v"(r) : "v"(a), "v"(b), "v"(c)); return r; }
DI float sw32max(float a) { auto r = __builtin_amdgcn_permlane32_swap(__float_as_uint(a), __float_as_uint(a), false, false); return fmaxf(__uint_as_float(r[0]), __uint_as_float(r[1])); }
DI float sw16max(float a) { auto r = __builtin_amdgcn_permlane16_swap(__float_as_uint(a), __float_as_uint(a), false, false); return fmaxf(__uint_as_float(r[0]), __uint_as_float(r[1])); }
DI float wave_sum(float v) { v += dpp<ROR8>(v); v += dpp<ROR4>(v); v += dpp<ROR2>(v); v += dpp<ROR1>(v); v = sw16add(v, v); return sw32add(v, v); }
DI float ex2(float v) { return __builtin_amdgcn_exp2f(v); }
DI float silu(float v) { return v * __builtin_amdgcn_rcpf(1.f + ex2(v * -1.4426950408889634f)); }
DI f32x4 mfma(v4u a, v4u b, f32x4 c) { return __builtin_amdgcn_mfma_f32_16x16x32_bf16(__builtin_bit_cast(bf16x8, a), __builtin_bit_cast(bf16x8, b), c, 0, 0, 0); }
DI v4u trrd(LAS char* p0, LAS char* p1) {
    const s16x4 lo = __builtin_amdgcn_ds_read_tr16_b64_v4i16((LAS s16x4*)p0), hi = __builtin_amdgcn_ds_read_tr16_b64_v4i16((LAS s16x4*)p1);
    return __builtin_bit_cast(v4u, __builtin_shufflevector(lo, hi, 0, 1, 2, 3, 4, 5, 6, 7));
}
#define BAR() do { asm volatile("s_waitcnt lgkmcnt(0)" ::: "memory"); __builtin_amdgcn_s_barrier(); asm volatile("" ::: "memory"); } while (0)
#define VMW(n) asm volatile("s_waitcnt vmcnt(%0)" :: "i"(n) : "memory")
#define CBAR() asm volatile("" ::: "memory")
#define SB() __builtin_amdgcn_sched_barrier(0)
#define ST(k) do { if ((k) == 0) { asm volatile("s_waitcnt lgkmcnt(0)" : "+s"(t0_) :: "memory"); tp_ = 0; } if ((k) == PROBE && (PROBEW == (w >= 4)) && (PROBEX < 0 || ((PROBEX >> (blockIdx.x & 7)) & 1))) { SB(); tp_ = __builtin_amdgcn_s_memrealtime(); asm volatile("s_waitcnt lgkmcnt(0)" ::: "memory"); SB(); } } while (0)
#define STOUT() do { if (tp_) { const u64 te_ = t0_ + 4000 + PROBEK * (tp_ - t0_); for (int g_ = 0; g_ < 1000000 && __builtin_amdgcn_s_memrealtime() < te_; ++g_) __builtin_amdgcn_s_sleep(1); } } while (0)
#define STV(k, v) (void)(v)
#define NTS ""
DI void dma16(unsigned m0v, unsigned voff, const char* base) {
    asm volatile("s_mov_b32 m0, %0\n\ts_nop 0\n\tglobal_load_lds_dwordx4 %1, %2" :: "s"(m0v), "v"(voff), "s"(base) : "memory");
}
template <int W, int R0, int R1> DI void issue_rows(const char* gbase, unsigned lds0, unsigned vbase) {
#pragma unroll
    for (int r = R0; r < R1; ++r) {
        const int i = r & 15, n = r >> 4;
        const unsigned voff = vbase ^ ((i << 4) | ((i >> 2) << 12) | (n << 16));
        if (r == R0 || (i & 3) == 0) {
            const unsigned m0v = lds0 + ((n & 1) << 16) + ((i >> 2) << 12);
            asm volatile("s_mov_b32 m0, %0\n\ts_waitcnt vmcnt(%3)\n\tglobal_load_lds_dwordx4 %1, %2 offset:%4" NTS
                         :: "s"(m0v), "v"(voff), "s"(gbase), "i"(W - 1), "i"((i & 3) * 1024) : "memory");
        } else {
            asm volatile("s_waitcnt vmcnt(%2)\n\tglobal_load_lds_dwordx4 %0, %1 offset:%3" NTS
                         :: "v"(voff), "s"(gbase), "i"(W - 1), "i"((i & 3) * 1024) : "memory");
        }
    }
}
DI unsigned gather8(__amdgpu_buffer_rsrc_t rs, int off, unsigned tag, LAS char* dst) {
    float c0, c1; unsigned spin;
    for (spin = 0;; ++spin) {
        CBAR();
        const v4u g0 = __builtin_amdgcn_raw_buffer_load_b128(rs, off, 0, SC1), g1 = __builtin_amdgcn_raw_buffer_load_b128(rs, off, 4096, SC1);
        const unsigned bad = ((g0.y ^ tag) | (g0.w ^ tag)) | ((g1.y ^ tag) | (g1.w ^ tag));
        c0 = __uint_as_float(g0.x) + __uint_as_float(g1.x); c1 = __uint_as_float(g0.z) + __uint_as_float(g1.z);
        if (__builtin_amdgcn_ballot_w64(bad != 0) == 0 || spin > SPIN_CAP) break;
        if (PSLP) __builtin_amdgcn_s_sleep(PSLP);
    }
    float cx = sw32add(c0, c1);
    cx = sw16add(cx, cx);
    *(LAS u16*)dst = (u16)pkbf(cx, cx);
    return spin;
}
}

__global__ void __launch_bounds__(512, 2)
mla_kernel(const u16* __restrict__ x, const char* __restrict__ ckv, const char* __restrict__ wq, const char* __restrict__ wuk,
           const char* __restrict__ wuv, const char* __restrict__ wo, const char* __restrict__ w1, const char* __restrict__ w2,
           unsigned* __restrict__ out, char* ws) {
    extern __shared__ __attribute__((aligned(16))) char smem_[];
    LAS char* L = (LAS char*)smem_;
    u64 t0_, tp_;
    asm volatile("s_memrealtime %0" : "=s"(t0_) :: "memory");
    SB();
    const char* pc;
    asm volatile("s_getpc_b64 %0" : "=s"(pc));
    pc = (const char*)((size_t)pc & ~(size_t)255);
    const int lane = threadIdx.x & 63, w = __builtin_amdgcn_readfirstlane(threadIdx.x >> 6), wa = w & 3;
    const bool isA = w >= 4;
    asm volatile("" :: "s"(x), "s"(ckv), "s"(wq), "s"(wuk), "s"(wuv), "s"(wo), "s"(w1), "s"(w2), "s"(out), "s"(ws));
    ST(0);
    const int b = blockIdx.x, xcd = b & 7, i8 = b >> 3, a = i8 & 1, ip = i8 >> 1;
    const int p = xcd * 16 + ip;
    const int hh = ip, h = 16 * a + hh, j = xcd;
    const int h2 = 4 * xcd + (i8 >> 3), j2 = i8 & 7;
    const int g = lane >> 4, rho = lane & 15;
    const unsigned lbase = (unsigned)(size_t)L;

    unsigned e0; v4u xv, xf0, xf1;
    {
        const unsigned o1 = lane * 16, o2 = 128 * j + 16 * g;
        asm volatile("global_load_dword %0, %4, %5 sc1\n\tglobal_load_dwordx4 %1, %6, %7\n\tglobal_load_dwordx4 %2, %8, %7\n\tglobal_load_dwordx4 %3, %8, %7 offset:64"
                     : "=&v"(e0), "=&v"(xv), "=&v"(xf0), "=&v"(xf1) : "v"(0), "s"(ws), "v"(o1), "s"(x), "v"(o2) : "memory");
    }
    const size_t wrow = ((size_t)(64 * j) * 4096 + 128 * h + 32 * wa) * 2;
    const unsigned wvoff = (lane >> 2) * 8192 + (((lane & 3) ^ ((lane >> 4) & 2)) << 4);
    const char* gbase = ckv + ((size_t)p * 256 + wa * 16) * 1024;
    {
        const char* sb = isA ? gbase : wq + wrow;
        const unsigned vb = isA ? (unsigned)lane << 4 : wvoff, xm = isA ? 16u : 0u, gs = isA ? 1024u : 131072u;
        const unsigned lb = lbase + (isA ? L_RING + wa * 16384 : L_Q + wa * 4096);
#pragma unroll
        for (int i = 0; i < 4; ++i) dma16(lb + i * 1024, (vb ^ (i * xm)) + i * gs, sb);
        dma16(lbase + L_JUNK, (unsigned)lane << 4, pc + ((w + 8 * (i8 & 1)) % CODE_KB) * 1024);
    }
    if (threadIdx.x < 8) *(volatile LAS unsigned*)(L + L_VF + 4 * lane) = 0u;
    BAR();
    const __amdgpu_buffer_rsrc_t rs = __builtin_amdgcn_make_buffer_rsrc(ws, 0, WS_END, RSRC_W3);
    const v4u ones = {0x3f803f80u, 0x3f803f80u, 0x3f803f80u, 0x3f803f80u};
    const int q4 = rho >> 2, p4 = lane & 3;
    const unsigned tb1 = wa * 4096 + (8 * g + q4) * 64 + (((p4 >> 1) ^ (2 * (g & 1))) << 4) + (p4 & 1) * 8;
    unsigned tag; float mean, rstd;
#define LN_STATS(NY) do { \
        asm volatile("s_waitcnt vmcnt(%4)" : "+v"(e0), "+v"(xv), "+v"(xf0), "+v"(xf1) : "i"(NY) : "memory"); \
        const float xf[8] = {bflo(xv.x), bfhi(xv.x), bflo(xv.y), bfhi(xv.y), bflo(xv.z), bfhi(xv.z), bflo(xv.w), bfhi(xv.w)}; \
        mean = wave_sum(((xf[0] + xf[1]) + (xf[2] + xf[3])) + ((xf[4] + xf[5]) + (xf[6] + xf[7]))) * (1.f / 512); \
        float vs = 0.f; \
        _Pragma("unroll") for (int i = 0; i < 8; ++i) { const float dd = xf[i] - mean; vs += dd * dd; } \
        rstd = __builtin_amdgcn_rsqf(wave_sum(vs) * (1.f / 512) + 1e-5f); \
        tag = __builtin_amdgcn_readfirstlane(e0) + 1u; \
        xf0 = rho == 0 ? xf0 : ones; xf1 = rho == 0 ? xf1 : ones; \
    } while (0)
    LN_STATS(5);

    if (!isA) {
        v4u wk[4];
        {
            const char* kb = wuk + ((size_t)(64 * j + 16 * wa) * 4096 + 128 * h) * 2;
            const unsigned ko = rho * 8192 + g * 16;
            asm volatile("global_load_dwordx4 %0, %4, %5\n\tglobal_load_dwordx4 %1, %4, %5 offset:64\n\tglobal_load_dwordx4 %2, %4, %5 offset:128\n\tglobal_load_dwordx4 %3, %4, %5 offset:192"
                         : "=&v"(wk[0]), "=&v"(wk[1]), "=&v"(wk[2]), "=&v"(wk[3]) : "v"(ko), "s"(kb) : "memory");
        }
        ST(1);
        VMW(5);
        {
            f32x4 c[2];
#pragma unroll
            for (int e = 0; e < 2; ++e) {
                LAS char* t = L + L_Q + (tb1 ^ (e * 32));
                c[e] = mfma(xf0, trrd(t, t + 256), f32x4{0.f, 0.f, 0.f, 0.f});
                c[e] = mfma(xf1, trrd(t + 2048, t + 2304), c[e]);
            }
            const v4u o = {__float_as_uint(rstd * (c[0].x - mean * c[0].y)), tag, __float_as_uint(rstd * (c[1].x - mean * c[1].y)), tag};
            asm volatile("s_waitcnt lgkmcnt(0)" ::: "memory");
            if (lane < 16) __builtin_amdgcn_raw_buffer_store_b128(o, rs, WS_QP + (h * 8 + j) * 1024 + (16 * wa + rho) * 16, 0, SC1);
        }
        ST(2);
        __builtin_amdgcn_s_sleep(SLP1);
        { const unsigned np = gather8(rs, WS_QP + (h * 8 + g) * 1024 + (16 * wa + rho) * 16, tag, L + L_QV + (32 * wa + 16 * (lane >> 5) + rho) * 2); STV(24, np); }
        ST(3);
        if (lane == 0) __hip_atomic_fetch_add((LAS unsigned*)(L + L_VF + 16), 1u, __ATOMIC_RELAXED, __HIP_MEMORY_SCOPE_WORKGROUP);
        {
            v4u qb[4];
            for (unsigned spin = 0;; ++spin) {
                CBAR();
                const unsigned cnt = *(volatile LAS unsigned*)(L + L_VF + 16);
                CBAR();
#pragma unroll
                for (int s = 0; s < 4; ++s) qb[s] = *(LAS v4u*)(L + L_QV + s * 64 + g * 16);
                CBAR();
                if (__builtin_amdgcn_readfirstlane(cnt) >= 4u || spin > SPIN_CAP * 16) break;
            }
            asm volatile("" : "+v"(wk[0]), "+v"(wk[1]), "+v"(wk[2]), "+v"(wk[3]));
            f32x4 cv = {0.f, 0.f, 0.f, 0.f};
#pragma unroll
            for (int s = 0; s < 4; ++s) cv = mfma(wk[s], qb[s], cv);
            cv *= 0.08838834764831845f * 1.4426950408889634f;
            const v4u o = {pkbf(cv.x, cv.y), pkbf(cv.z, cv.w), tag, ~tag};
            if (rho == 0) __builtin_amdgcn_raw_buffer_store_b128(o, rs, WS_QA + (a * 128 + hh * 8 + j) * 256 + (wa * 4 + g) * 16, 0, SC1);
        }
        ST(4);
        const int z = 8 * (g & 1) + 2 * q4 + (g >> 1);
        unsigned ta[8];
#pragma unroll
        for (int t = 0; t < 8; ++t) { ta[t] = lbase + z * 1024 + 256 * wa + 16 * ((2 * t + (p4 >> 1)) ^ z) + 8 * (p4 & 1); asm volatile("" : "+v"(ta[t])); }
        f32x4 acc[8], lacc = {0.f, 0.f, 0.f, 0.f};
#pragma unroll
        for (int t = 0; t < 8; ++t) { acc[t] = f32x4{0.f, 0.f, 0.f, 0.f}; asm volatile("" : "+v"(acc[t])); }
        __builtin_amdgcn_s_sleep(SLP2);
        {
            const int go = WS_QA + (a * 128 + wa * 32) * 256 + lane * 16;
            LAS char* qd = L + L_Q + ((2 * g + (p4 >> 1)) * 64 + q4 * 16 + 4 * wa) * 16 + (p4 & 1) * 8;
            for (unsigned spin = 0;; ++spin) {
                CBAR();
                v4u gq[8];
#pragma unroll
                for (int k = 0; k < 8; ++k) gq[k] = __builtin_amdgcn_raw_buffer_load_b128(rs, go + (k & 3) * 1024, (k >> 2) * 4096, SC1);
                unsigned bad = 0;
#pragma unroll
                for (int k = 0; k < 8; ++k) {
                    bad |= (gq[k].z ^ tag) | (gq[k].w ^ ~tag);
                    *(LAS v2u*)(qd + (k & 1) * 8192 + (k >> 1) * 16) = v2u{gq[k].x, gq[k].y};
                }
                if (__builtin_amdgcn_ballot_w64(bad != 0) == 0 || spin > SPIN_CAP) { STV(25, spin); break; }
                if (PSLP) __builtin_amdgcn_s_sleep(PSLP);
            }
        }
        ST(5);
        BAR();
        float mref = -1e30f;
#pragma nounroll
        for (int n = 0; n < 4; ++n) {
            v4u tv[2][8];
#pragma unroll
            for (int ks = 0; ks < 2; ++ks)
#pragma unroll
                for (int t = 0; t < 8; ++t) tv[ks][t] = trrd((LAS char*)(size_t)ta[t] + ks * 32768, (LAS char*)(size_t)ta[t] + ks * 32768 + 16384);
#pragma unroll
            for (int t = 0; t < 8; ++t) ta[t] ^= 65536u;
            BAR();
            const unsigned vf = *(volatile LAS unsigned*)(L + L_VF + 4 * n);
            v4u pf0 = *(LAS v4u*)(L + L_PX + lane * 16), pf1 = *(LAS v4u*)(L + L_PX + 1024 + lane * 16);
            if (__builtin_amdgcn_readfirstlane(vf) != 0) {
                BAR();
                const f32x4 m4 = *(LAS f32x4*)(L + L_MX + rho * 16);
                const float mn = fmaxf(fmaxf(fmaxf(m4.x, m4.y), fmaxf(m4.z, m4.w)), mref);
                const float al = ex2(mref - mn);
                mref = mn;
#pragma unroll
                for (int t = 0; t < 8; ++t) acc[t] *= al;
                lacc *= al;
                BAR();
                pf0 = *(LAS v4u*)(L + L_PX + lane * 16); pf1 = *(LAS v4u*)(L + L_PX + 1024 + lane * 16);
            }
#pragma unroll
            for (int t = 0; t < 8; ++t) acc[t] = mfma(tv[0][t], pf0, acc[t]);
            lacc = mfma(ones, pf0, lacc);
            lacc = mfma(ones, pf1, lacc);
#pragma unroll
            for (int t = 0; t < 8; ++t) acc[t] = mfma(tv[1][t], pf1, acc[t]);
            if (n == 0) ST(6); else if (n == 1) ST(7); else if (n == 2) ST(8); else ST(9);
            if (n < 3) BAR();
        }
        const int ob = WS_OP + (a * 128 + p) * OSTR;
        v4u o[4];
#pragma unroll
        for (int k = 0; k < 4; ++k) {
            const f32x4 t0 = acc[2 * k], t1 = acc[2 * k + 1];
            o[k] = v4u{pkbf(t0.x, t0.y), pkbf(t0.z, t0.w), pkbf(t1.x, t1.y), pkbf(t1.z, t1.w)};
            __builtin_amdgcn_raw_buffer_store_b128(o[k], rs, wa * 4096 + rho * 64 + g * 16 + k * 1024, ob, SC1);
        }
        SB();
        {
            const unsigned c0 = (o[0].x + o[0].y + o[0].z) + (o[0].w + o[1].x + o[1].y) + (o[1].z + o[1].w);
            const unsigned c1 = (o[2].x + o[2].y + o[2].z) + (o[2].w + o[3].x + o[3].y) + (o[3].z + o[3].w);
            auto r1 = __builtin_amdgcn_permlane32_swap(c0, c1, false, false); unsigned s1 = r1[0] + r1[1];
            auto r3 = __builtin_amdgcn_permlane16_swap(s1, s1, false, false); s1 = r3[0] + r3[1];
            const unsigned mb = __float_as_uint(mref), lb = __float_as_uint(lacc.x);
            const v4u hd = {mb, lb, s1 + mb + lb, tag};
            if ((lane & 16) == 0) __builtin_amdgcn_raw_buffer_store_b128(hd, rs, 16384 + (2 * wa + (lane >> 5)) * 256 + rho * 16, ob, SC1);
        }
        ST(10);
        __builtin_amdgcn_s_sleep(SLP3);
    } else {
        const unsigned lds0 = lbase + L_RING + wa * 16384;
        const unsigned vbase = lane << 4;
        ST(1);
        issue_rows<W1, 4, 32>(gbase, lds0, vbase);
        const int kap = 8 * (q4 & 1) + 2 * (rho & 3) + (rho >> 3);
        unsigned sa[4];
#pragma unroll
        for (int s = 0; s < 4; ++s) { sa[s] = lbase + (16 * wa + kap) * 1024 + 16 * ((8 * (s >> 1) + 2 * g + (s & 1)) ^ kap); asm volatile("" : "+v"(sa[s])); }
        LAS v2u* pxw = (LAS v2u*)(L + L_PX + (wa >> 1) * 1024 + lane * 16 + (wa & 1) * 8);
        __builtin_amdgcn_s_setprio(1);
        ST(2);
        BAR();
        ST(3);
        v4u qf[16];
#pragma unroll
        for (int s = 0; s < 16; ++s) qf[s] = *(LAS v4u*)(L + L_Q + s * 1024 + lane * 16);
        float mref = -1e30f;
        const int U0 = (43 * h) >> 3, un = ((43 * (h + 1)) >> 3) - U0;
#pragma nounroll
        for (int n = 0;; ++n) {
            f32x4 sv = {0.f, 0.f, 0.f, 0.f};
            {
                v4u af[16];
#pragma unroll
                for (int s = 0; s < 16; ++s) af[s] = *(LAS v4u*)((LAS char*)(size_t)sa[s & 3] + (s >> 2) * 256);
                SB();
#pragma unroll
                for (int s = 0; s < 16; ++s) sv = mfma(af[s], qf[s], sv);
            }
            f32x4 d = sv - mref;
            *pxw = v2u{pkbf(ex2(d.x), ex2(d.y)), pkbf(ex2(d.z), ex2(d.w))};
            if (!(vmax(vmax3(d.x, d.y, d.z), d.w) <= 10.f)) *(volatile LAS unsigned*)(L + L_VF + 4 * n) = 1u;
            BAR();
            if (__builtin_amdgcn_readfirstlane(*(volatile LAS unsigned*)(L + L_VF + 4 * n)) != 0) {
                float tm = fmaxf(fmaxf(sv.x, sv.y), fmaxf(sv.z, sv.w));
                tm = sw32max(sw16max(tm));
                if (lane < 16) *(LAS float*)(L + L_MX + lane * 16 + wa * 4) = tm;
                BAR();
                const f32x4 m4 = *(LAS f32x4*)(L + L_MX + rho * 16);
                mref = fmaxf(fmaxf(fmaxf(m4.x, m4.y), fmaxf(m4.z, m4.w)), mref);
                d = sv - mref;
                *pxw = v2u{pkbf(ex2(d.x), ex2(d.y)), pkbf(ex2(d.z), ex2(d.w))};
                BAR();
            }
            if (n == 0) ST(4); else if (n == 1) ST(6); else if (n == 2) ST(8); else ST(10);
            if (n == 3) break;
#pragma unroll
            for (int s = 0; s < 4; ++s) sa[s] ^= 65536u;
            if (n == 0) { issue_rows<W2, 32, 32 + W2>(gbase, lds0, vbase); VMW(W2); }
            else if (n == 1) { issue_rows<W2, 32 + W2, 64>(gbase, lds0, vbase); }
            else {
#pragma unroll
                for (int i = 0; i < 2; ++i) {
                    const int uc = 2 * wa + (lane & 1);
                    dma16(lbase + L_W1 + wa * 2048 + i * 1024, (32 * i + (lane >> 1)) * (F * 2) + (uc < un ? uc : 0) * 16, w1 + (size_t)(64 * j) * (F * 2) + U0 * 16);
                }
#pragma unroll
                for (int i = 0; i < 4; ++i) dma16(lbase + L_WV + wa * 4096 + i * 1024, wvoff + i * 131072, wuv + wrow);
                const int r8 = lane >> 3;
#pragma unroll
                for (int i = 0; i < 4; ++i)
                    dma16(lbase + L_WO + (4 * wa + i) * 1024, (8 * i + r8) * 1024 + (((lane & 7) ^ ((r8 & 2) + 4 * (i & 1))) << 4), wo + ((size_t)(h2 * 128 + 32 * wa) * 512 + 64 * j2) * 2);
#pragma unroll
                for (int i = 0; i < 2; ++i) {
                    const int k = 2 * wa + i;
                    const int V0 = (43 * h2) >> 3, vn = ((43 * (h2 + 1)) >> 3) - V0;
                    dma16(lbase + L_W2 + k * 1024, r8 * 1024 + (((lane & 7) ^ ((r8 & 2) + 4 * (i & 1))) << 4), w2 + (size_t)(8 * (V0 + (k < vn ? k : 0))) * 1024 + 128 * j2);
                }
                VMW(12);
            }
            if (n == 0) ST(5); else if (n == 1) ST(7); else ST(9);
            BAR();
        }
        __builtin_amdgcn_s_setprio(0);
        VMW(10);
        {
            LAS char* t = L + L_W1 + wa * 2048 + (8 * g + q4) * 32 + p4 * 8;
            f32x4 c = mfma(xf0, trrd(t, t + 128), f32x4{0.f, 0.f, 0.f, 0.f});
            c = mfma(xf1, trrd(t + 1024, t + 1152), c);
            if (lane < 16) __builtin_amdgcn_raw_buffer_store_b64(v2u{__float_as_uint(rstd * (c.x - mean * c.y)), tag}, rs, WS_HP + (h * 8 + j) * 512 + wa * 128 + rho * 8, 0, SC1);
        }
        VMW(1);
        ST(11);
    }
    BAR();
    ST(12);
    v4u bv[4], bo[4], by[2];
    {
        const int msg = lane >> 3, pc8 = lane & 7;
        const int base = WS_OP + (a * 128 + w * 16 + msg) * OSTR;
        const int po = base + (j * 2 + (pc8 >> 2)) * 1024 + hh * 64 + (pc8 & 3) * 16, ho = base + 16384 + j * 256 + hh * 16;
        v4u pl[2], hd[2];
#pragma unroll
        for (int k = 0; k < 2; ++k) {
            pl[k] = __builtin_amdgcn_raw_buffer_load_b128(rs, po, k * 8 * OSTR, SC1);
            hd[k] = __builtin_amdgcn_raw_buffer_load_b128(rs, ho, k * 8 * OSTR, SC1);
        }
        SB();
#pragma unroll
        for (int e = 0; e < 2; ++e) { LAS char* t = L + L_WV + (tb1 ^ (e * 32)); bv[2 * e] = trrd(t, t + 256); bv[2 * e + 1] = trrd(t + 2048, t + 2304); }
        {
            const unsigned cb = (((2 * wa + (p4 >> 1)) ^ ((q4 & 2) + 4 * (g & 1))) << 4) + (p4 & 1) * 8;
            LAS char* t = L + L_WO + (8 * g + q4) * 128 + cb;
#pragma unroll
            for (int s = 0; s < 4; ++s) bo[s] = trrd(t + s * 4096, t + s * 4096 + 512);
            by[0] = trrd(t + (L_W2 - L_WO), t + (L_W2 - L_WO) + 512);
            LAS char* t2 = L + L_W2 + 4096 + (8 * (g & 1) + q4) * 128 + cb;
            by[1] = trrd(t2, t2 + 512);
        }
        SB();
        for (unsigned spin = 0;; ++spin) {
            bool ok[2];
#pragma unroll
            for (int k = 0; k < 2; ++k) {
                unsigned s1 = (pl[k].x + pl[k].y + pl[k].z) + pl[k].w;
                s1 += dppu<QX1>(s1); s1 += dppu<QX2>(s1); s1 += dppu<HMIR>(s1);
                ok[k] = (s1 + hd[k].x + hd[k].y == hd[k].z) & (hd[k].w == tag);
            }
            if (__builtin_amdgcn_ballot_w64(!(ok[0] & ok[1])) == 0 || spin > SPIN_CAP) { STV(26, spin); break; }
            if (PSLP) __builtin_amdgcn_s_sleep(PSLP);
            CBAR();
#pragma unroll
            for (int k = 0; k < 2; ++k) if (!ok[k]) {
                pl[k] = __builtin_amdgcn_raw_buffer_load_b128(rs, po, k * 8 * OSTR, SC1);
                hd[k] = __builtin_amdgcn_raw_buffer_load_b128(rs, ho, k * 8 * OSTR, SC1);
            }
        }
        ST(13);
        const float m0 = __uint_as_float(hd[0].x), m1 = __uint_as_float(hd[1].x);
        float mw = fmaxf(m0, m1);
        mw = fmaxf(mw, dpp<ROR8>(mw)); mw = sw32max(sw16max(mw));
        const float f0 = ex2(m0 - mw), f1 = ex2(m1 - mw);
        float a8[8];
        a8[0] = f0 * bflo(pl[0].x) + f1 * bflo(pl[1].x); a8[1] = f0 * bfhi(pl[0].x) + f1 * bfhi(pl[1].x);
        a8[2] = f0 * bflo(pl[0].y) + f1 * bflo(pl[1].y); a8[3] = f0 * bfhi(pl[0].y) + f1 * bfhi(pl[1].y);
        a8[4] = f0 * bflo(pl[0].z) + f1 * bflo(pl[1].z); a8[5] = f0 * bfhi(pl[0].z) + f1 * bfhi(pl[1].z);
        a8[6] = f0 * bflo(pl[0].w) + f1 * bflo(pl[1].w); a8[7] = f0 * bfhi(pl[0].w) + f1 * bfhi(pl[1].w);
        const float v0 = sw32add(a8[0], a8[4]), v1 = sw32add(a8[1], a8[5]), v2 = sw32add(a8[2], a8[6]), v3 = sw32add(a8[3], a8[7]);
        float u0 = sw16add(v0, v2), u1 = sw16add(v1, v3);
        u0 += dpp<ROR8>(u0); u1 += dpp<ROR8>(u1);
        *(LAS unsigned*)(L + L_PART + w * 128 + (16 * (2 * (pc8 >> 2) + (lane >> 5)) + 4 * (pc8 & 3) + 2 * (g & 1)) * 2) = pkbf(u0, u1);
        float lw = f0 * __uint_as_float(hd[0].y) + f1 * __uint_as_float(hd[1].y);
        lw += dpp<ROR8>(lw); lw = sw16add(lw, lw); lw = sw32add(lw, lw);
        *(LAS float*)(L + L_ML + w * 4 + (lane & 1) * 32) = (lane & 1) ? lw : mw;
    }
    BAR();
    if (!isA) { STOUT(); return; }
    const int un = ((43 * (h2 + 1)) >> 3) - ((43 * h2) >> 3);
    const int sl = lane & 31, hoff = WS_HP + (h2 * 8 + (lane >> 5)) * 512 + (sl < 24 ? sl : 0) * 16;
    v4u gh[4];
#pragma unroll
    for (int k = 0; k < 4; ++k) gh[k] = __builtin_amdgcn_raw_buffer_load_b128(rs, hoff + k * 1024, 0, SC1);
    {
        const v4u a0 = *(LAS v4u*)(L + L_PART + (rho & 7) * 128 + g * 16), a1 = *(LAS v4u*)(L + L_PART + (rho & 7) * 128 + 64 + g * 16);
        const f32x4 ma = *(LAS f32x4*)(L + L_ML + (g & 1) * 16), mb = *(LAS f32x4*)(L + L_ML + 16 - (g & 1) * 16), la = *(LAS f32x4*)(L + L_ML + 32 + (g & 1) * 16);
        f32x4 c0 = mfma(a0, bv[0], f32x4{0.f, 0.f, 0.f, 0.f}), c1 = mfma(a0, bv[2], f32x4{0.f, 0.f, 0.f, 0.f});
        c0 = mfma(a1, bv[1], c0); c1 = mfma(a1, bv[3], c1);
        const float mx = vmax3(vmax3(ma.x, ma.y, ma.z), vmax3(ma.w, mb.x, mb.y), vmax(mb.z, mb.w));
        const f32x4 f = {ex2(ma.x - mx), ex2(ma.y - mx), ex2(ma.z - mx), ex2(ma.w - mx)};
        float den = (f.x * la.x + f.y * la.y) + (f.z * la.z + f.w * la.w);
        den = __builtin_amdgcn_rcpf(sw16add(den, den));
        float r0 = (f.x * c0.x + f.y * c0.y) + (f.z * c0.z + f.w * c0.w), r1 = (f.x * c1.x + f.y * c1.y) + (f.z * c1.z + f.w * c1.w);
        r0 = sw16add(r0, r0) * den; r1 = sw16add(r1, r1) * den;
        const v4u o = {__float_as_uint(r0), tag, __float_as_uint(r1), tag};
        if (lane < 16) __builtin_amdgcn_raw_buffer_store_b128(o, rs, WS_CP + (h * 8 + j) * 1024 + (16 * wa + rho) * 16, 0, SC1);
    }
    ST(14);
    SB();
    float yp;
    {
        for (unsigned spin = 0;; ++spin) {
            unsigned bad = 0;
#pragma unroll
            for (int k = 0; k < 4; ++k) bad |= (gh[k].y ^ tag) | (gh[k].w ^ tag);
            if (__builtin_amdgcn_ballot_w64(bad != 0) == 0 || spin > SPIN_CAP) break;
            CBAR();
#pragma unroll
            for (int k = 0; k < 4; ++k) gh[k] = __builtin_amdgcn_raw_buffer_load_b128(rs, hoff + k * 1024, 0, SC1);
        }
        const float h0 = (__uint_as_float(gh[0].x) + __uint_as_float(gh[1].x)) + (__uint_as_float(gh[2].x) + __uint_as_float(gh[3].x));
        const float h1 = (__uint_as_float(gh[0].z) + __uint_as_float(gh[1].z)) + (__uint_as_float(gh[2].z) + __uint_as_float(gh[3].z));
        float hv = silu(sw32add(h0, h1));
        hv = sl < 4 * un ? hv : 0.f;
        *(LAS u16*)(L + L_HV + wa * 128 + (2 * sl + (lane >> 5)) * 2) = (u16)pkbf(hv, hv);
        asm volatile("s_waitcnt lgkmcnt(0)" ::: "memory");
        f32x4 c = mfma(*(LAS v4u*)(L + L_HV + wa * 128 + g * 16), by[0], f32x4{0.f, 0.f, 0.f, 0.f});
        c = mfma(*(LAS v4u*)(L + L_HV + wa * 128 + 64 + g * 16), by[1], c);
        yp = c.x;
    }
    const unsigned xo = *(const GAS unsigned*)(x + 64 * j2 + 2 * h2);
    ST(15);
    SB();
    { const unsigned np = gather8(rs, WS_CP + (h2 * 8 + g) * 1024 + (16 * wa + rho) * 16, tag, L + L_QV + (32 * wa + 16 * (lane >> 5) + rho) * 2); STV(27, np); }
    ST(16);
    BAR();
    {
        f32x4 c = {0.f, 0.f, 0.f, 0.f};
#pragma unroll
        for (int s = 0; s < 4; ++s) c = mfma(*(LAS v4u*)(L + L_QV + s * 64 + g * 16), bo[s], c);
        const v4u o = {__float_as_uint(c.x), __float_as_uint(yp), tag, ~tag};
        if (lane < 16) __builtin_amdgcn_raw_buffer_store_b128(o, rs, WS_FP + (j2 * 32 + h2) * 1024 + (16 * wa + rho) * 16, 0, SC1);
    }
    ST(17);
    if (wa != 0) { STOUT(); return; }
    {
        __builtin_amdgcn_s_sleep(SLP5);
        v4u gf;
        for (unsigned spin = 0;; ++spin) {
            CBAR();
            gf = __builtin_amdgcn_raw_buffer_load_b128(rs, WS_FP + (j2 * 32 + (lane >> 1)) * 1024 + (2 * h2 + (lane & 1)) * 16, 0, SC1);
            if (__builtin_amdgcn_ballot_w64(((gf.z ^ tag) | (gf.w ^ ~tag)) != 0) == 0 || spin > SPIN_CAP) { STV(28, spin); break; }
        }
        float v = sw32add(__uint_as_float(gf.x), __uint_as_float(gf.y));
        v = sw16add(v, v); v += dpp<ROR8>(v); v += dpp<ROR4>(v); v += dpp<ROR2>(v);
        auto r = __builtin_amdgcn_permlane32_swap(__float_as_uint(v), __float_as_uint(v), false, false);
        const float xe = (lane & 1) ? bfhi(xo) : bflo(xo);
        const float res = xe + __uint_as_float(r[0]) * silu(__uint_as_float(r[1]));
        const float other = dpp<QX1>(res);
        if (lane == 0) {
            out[32 * j2 + h2] = pkbf(res, other);
            if (b == 0) __hip_atomic_store((GAS unsigned*)(ws + WS_E), tag, __ATOMIC_RELAXED, __HIP_MEMORY_SCOPE_AGENT);
        }
        ST(18);
    }
    STOUT();
}

extern "C" void kernel_launch(void* const* d_in, const int* in_sizes, int n_in, void* d_out, int out_size, void* d_ws, size_t ws_size, hipStream_t stream) {
    (void)in_sizes; (void)n_in; (void)out_size; (void)ws_size;
    (void)hipFuncSetAttribute((const void*)mla_kernel, hipFuncAttributeMaxDynamicSharedMemorySize, L_END);
    mla_kernel<<<256, 512, L_END, stream>>>((const u16*)d_in[0], (const char*)d_in[1], (const char*)d_in[2], (const char*)d_in[3], (const char*)d_in[4],
                                            (const char*)d_in[5], (const char*)d_in[7], (const char*)d_in[8], (unsigned*)d_out, (char*)d_ws);
}
```

```cpp
#include <hip/hip_runtime.h>
#define WS_DBG 8388608
#define STAMPS 0

__global__ void __launch_bounds__(512, 2)
mla_kernel(const void* x, void* ws, const void* wq, const void* ckv, const void* wuk, const void* wuv, const void* w1,
           const void* wo, const void* w2, void* out) {
    asm volatile(
        "s_mov_b32 s74, %1\n"
        "s_load_dwordx2 s[2:3], %0, 0x0\n"
        "s_load_dwordx8 s[4:11], %0, 0x8\n"
        "s_load_dwordx4 s[12:15], %0, 0x28\n"
        "s_getpc_b64 s[26:27]\n"
        "v_readfirstlane_b32 s28, %2\n"
        "v_and_b32 v1, 63, %2\n"
        "s_and_b32 s26, s26, 0xffffff00\n"
        "s_lshr_b32 s28, s28, 6\n"
        "v_lshlrev_b32 v2, 4, v1\n"
        "s_and_b32 s42, s74, 8\n"
        "s_add_u32 s42, s42, s28\n"
        "s_min_u32 s42, s42, 12\n"
        "s_lshl_b32 s42, s42, 10\n"
        "s_mov_b32 m0, 8192\n"
        "s_add_u32 s26, s26, s42\n"
        "s_addc_u32 s27, s27, 0\n"
        "global_load_lds_dwordx4 v2, s[26:27]\n"
        "v_mov_b32 v12, 0\n"
        "v_lshlrev_b32 v40, 2, v1\n"
        "ds_write_b32 v40, v12 offset:0\n"
        "s_and_b32 s29, s28, 3\n"
        "s_and_b32 s43, s74, 7\n"
        "s_lshr_b32 s44, s74, 4\n"
        "s_lshl_b32 s36, s43, 4\n"
        "s_add_u32 s36, s36, s44\n"
        "s_mov_b32 s30, s43\n"
        "s_mov_b32 s34, s44\n"
        "s_bfe_u32 s35, s74, 0x10003\n"
        "s_lshl_b32 s31, s35, 4\n"
        "s_add_u32 s31, s31, s34\n"
        "v_and_b32 v3, 48, v1\n"
        "v_and_b32 v4, 15, v1\n"
        "v_and_b32 v6, 3, v1\n"
        "v_bfe_u32 v5, v1, 2, 2\n"
        "s_lshl_b32 s43, s30, 7\n"
        "v_or_b32 v41, s43, v3\n"
        "s_lshl_b32 s70, s30, 19\n"
        "s_lshl_b32 s44, s31, 8\n"
        "s_add_u32 s45, s70, s44\n"
        "s_lshl_b32 s44, s29, 6\n"
        "s_add_u32 s70, s45, s44\n"
        "v_lshrrev_b32 v42, 2, v1\n"
        "v_lshrrev_b32 v43, 4, v1\n"
        "v_bitop3_b32 v43, v43, v6, 2 bitop3:0x6c\n"
        "v_lshlrev_b32 v43, 4, v43\n"
        "v_lshl_or_b32 v8, v42, 13, v43\n"
        "v_add_u32 v9, 0x20000, v8\n"
        "v_add_u32 v10, 0x40000, v8\n"
        "v_add_u32 v11, 0x60000, v8\n"
        "s_lshl_b32 s46, s29, 12\n"
        "s_add_u32 m0, s46, 16384\n"
        "v_mov_b32 v16, 0x3f803f80\n"
        "s_cmp_ge_u32 s28, 4\n"
        "s_waitcnt lgkmcnt(0)\n"
        "s_barrier\n"
        "global_load_dword v13, v12, s[4:5] sc1\n"
        "global_load_dwordx4 v[20:23], v2, s[2:3]\n"
        "global_load_dwordx4 v[24:27], v41, s[2:3]\n"
        "global_load_dwordx4 v[28:31], v41, s[2:3] offset:64\n"
        "s_cbranch_scc1 APATH\n"
        "s_add_u32 s50, s6, s70\n"
        "s_addc_u32 s51, s7, 0\n"
        "global_load_lds_dwordx4 v8, s[50:51]\n"
        "s_add_u32 m0, m0, 0x400\n"
        "s_nop 0\n"
        "global_load_lds_dwordx4 v9, s[50:51]\n"
        "s_add_u32 m0, m0, 0x400\n"
        "s_nop 0\n"
        "global_load_lds_dwordx4 v10, s[50:51]\n"
        "s_add_u32 m0, m0, 0x400\n"
        "s_nop 0\n"
        "global_load_lds_dwordx4 v11, s[50:51]\n"
        "s_memrealtime s[88:89]\n"
        "s_load_dwordx4 s[20:23], %0, 0x38\n"
        "s_load_dwordx2 s[24:25], %0, 0x48\n"
        "v_mov_b32 v17, v16\n"
        "v_mov_b32 v18, v16\n"
        "v_mov_b32 v19, v16\n"
        "v_lshrrev_b32 v44, 1, v3\n"
        "v_or_b32 v44, v44, v5\n"
        "v_lshlrev_b32 v45, 3, v6\n"
        "v_lshlrev_b32 v46, 1, v3\n"
        "v_and_b32 v46, 32, v46\n"
        "v_xor_b32 v45, v45, v46\n"
        "v_lshl_or_b32 v7, v44, 6, v45\n"
        "v_or_b32 v7, s46, v7\n"
        "v_lshl_or_b32 v116, v4, 13, v3\n"
        "s_lshl_b32 s44, s29, 17\n"
        "s_add_u32 s44, s44, s45\n"
        "s_add_u32 s48, s10, s44\n"
        "s_addc_u32 s49, s11, 0\n"
        "v_xor_b32 v124, 32, v7\n"
        "s_lshl_b32 s42, s31, 13\n"
        "s_add_u32 s42, s42, 4096\n"
        "s_add_u32 s50, s4, s42\n"
        "s_addc_u32 s51, s5, 0\n"
        "s_lshl_b32 s42, s30, 10\n"
        "s_lshl_b32 s43, s29, 8\n"
        "s_add_u32 s42, s42, s43\n"
        "v_lshl_add_u32 v117, v4, 4, s42\n"
        "v_cmp_eq_u32 vcc, 0, v4\n"
        "s_waitcnt vmcnt(4)\n"
        "v_dot2_f32_bf16 v14, v20, v16, 0\n"
        "v_dot2_f32_bf16 v14, v21, v16, v14\n"
        "v_dot2_f32_bf16 v14, v22, v16, v14\n"
        "v_dot2_f32_bf16 v14, v23, v16, v14\n"
        "v_readfirstlane_b32 s38, v13\n"
        "v_cndmask_b32 v24, v16, v24, vcc\n"
        "v_cndmask_b32 v25, v16, v25, vcc\n"
        "v_add_f32_dpp v14, v14, v14 row_ror:8 row_mask:0xf bank_mask:0xf\n"
        "v_cndmask_b32 v26, v16, v26, vcc\n"
        "v_cndmask_b32 v27, v16, v27, vcc\n"
        "v_add_f32_dpp v14, v14, v14 row_ror:4 row_mask:0xf bank_mask:0xf\n"
        "v_cndmask_b32 v28, v16, v28, vcc\n"
        "v_cndmask_b32 v29, v16, v29, vcc\n"
        "v_add_f32_dpp v14, v14, v14 row_ror:2 row_mask:0xf bank_mask:0xf\n"
        "v_cndmask_b32 v30, v16, v30, vcc\n"
        "v_cndmask_b32 v31, v16, v31, vcc\n"
        "v_add_f32_dpp v14, v14, v14 row_ror:1 row_mask:0xf bank_mask:0xf\n"
        "v_mov_b32 v47, v14\n"
        "s_add_u32 s38, s38, 1\n"
        "s_not_b32 s39, s38\n"
        "v_permlane16_swap_b32 v14, v47\n"
        "v_add_f32 v14, v14, v47\n"
        "v_mov_b32 v47, v14\n"
        "s_nop 1\n"
        "v_permlane32_swap_b32 v14, v47\n"
        "v_add_f32 v14, v14, v47\n"
        "v_mul_f32 v14, 0x3b000000, v14\n"
        "v_mov_b32 v121, s38\n"
        "v_mov_b32 v123, s38\n"
        "s_waitcnt vmcnt(0) lgkmcnt(0)\n"
        "ds_read_b64_tr_b16 v[72:73], v7 offset:16384\n"
        "ds_read_b64_tr_b16 v[74:75], v7 offset:16640\n"
        "ds_read_b64_tr_b16 v[76:77], v7 offset:18432\n"
        "ds_read_b64_tr_b16 v[78:79], v7 offset:18688\n"
        "ds_read_b64_tr_b16 v[80:81], v124 offset:16384\n"
        "ds_read_b64_tr_b16 v[82:83], v124 offset:16640\n"
        "ds_read_b64_tr_b16 v[84:85], v124 offset:18432\n"
        "ds_read_b64_tr_b16 v[86:87], v124 offset:18688\n"
        "s_waitcnt lgkmcnt(6)\n"
        "v_mfma_f32_16x16x32_bf16 v[64:67], v[24:27], v[72:75], 0\n"
        "s_waitcnt lgkmcnt(4)\n"
        "v_mfma_f32_16x16x32_bf16 v[64:67], v[28:31], v[76:79], v[64:67]\n"
        "s_waitcnt lgkmcnt(2)\n"
        "v_mfma_f32_16x16x32_bf16 v[68:71], v[24:27], v[80:83], 0\n"
        "s_waitcnt lgkmcnt(0)\n"
        "v_mfma_f32_16x16x32_bf16 v[68:71], v[28:31], v[84:87], v[68:71]\n"
        "s_mov_b64 exec, 0xffff\n"
        "s_nop 2\n"
        "v_fma_f32 v120, -v14, v65, v64\n"
        "s_nop 2\n"
        "v_fma_f32 v122, -v14, v69, v68\n"
        "global_store_dwordx4 v117, v[120:123], s[50:51] sc1\n"
        "s_mov_b64 exec, -1\n"
        "global_load_dwordx4 v[128:131], v116, s[48:49] offset:0\n"
        "global_load_dwordx4 v[132:135], v116, s[48:49] offset:64\n"
        "global_load_dwordx4 v[136:139], v116, s[48:49] offset:128\n"
        "global_load_dwordx4 v[140:143], v116, s[48:49] offset:192\n"
        "s_lshl_b32 s42, s29, 8\n"
        "s_add_u32 s42, s42, 2048\n"
        "v_lshlrev_b32 v46, 4, v4\n"
        "v_lshl_or_b32 v46, v3, 6, v46\n"
        "v_add_u32 v32, s42, v46\n"
        "v_and_b32 v46, 32, v1\n"
        "v_lshl_or_b32 v46, v4, 1, v46\n"
        "s_lshl_b32 s42, s29, 6\n"
        "v_or_b32 v33, s42, v46\n"
        "v_lshlrev_b32 v88, 16, v20\n"
        "v_and_b32 v89, 0xffff0000, v20\n"
        "v_lshlrev_b32 v90, 16, v21\n"
        "v_and_b32 v91, 0xffff0000, v21\n"
        "v_lshlrev_b32 v92, 16, v22\n"
        "v_and_b32 v93, 0xffff0000, v22\n"
        "v_lshlrev_b32 v94, 16, v23\n"
        "v_and_b32 v95, 0xffff0000, v23\n"
        "v_sub_f32 v88, v88, v14\n"
        "v_sub_f32 v89, v89, v14\n"
        "v_sub_f32 v90, v90, v14\n"
        "v_sub_f32 v91, v91, v14\n"
        "v_sub_f32 v92, v92, v14\n"
        "v_sub_f32 v93, v93, v14\n"
        "v_sub_f32 v94, v94, v14\n"
        "v_sub_f32 v95, v95, v14\n"
        "v_mul_f32 v15, v88, v88\n"
        "v_fmac_f32 v15, v89, v89\n"
        "v_fmac_f32 v15, v90, v90\n"
        "v_fmac_f32 v15, v91, v91\n"
        "v_fmac_f32 v15, v92, v92\n"
        "v_fmac_f32 v15, v93, v93\n"
        "v_fmac_f32 v15, v94, v94\n"
        "v_fmac_f32 v15, v95, v95\n"
        "s_nop 1\n"
        "v_add_f32_dpp v15, v15, v15 row_ror:8 row_mask:0xf bank_mask:0xf\n"
        "s_nop 1\n"
        "v_add_f32_dpp v15, v15, v15 row_ror:4 row_mask:0xf bank_mask:0xf\n"
        "s_nop 1\n"
        "v_add_f32_dpp v15, v15, v15 row_ror:2 row_mask:0xf bank_mask:0xf\n"
        "s_nop 1\n"
        "v_add_f32_dpp v15, v15, v15 row_ror:1 row_mask:0xf bank_mask:0xf\n"
        "v_mov_b32 v88, v15\n"
        "s_nop 1\n"
        "v_permlane16_swap_b32 v15, v88\n"
        "v_add_f32 v15, v15, v88\n"
        "v_mov_b32 v88, v15\n"
        "s_nop 1\n"
        "v_permlane32_swap_b32 v15, v88\n"
        "v_add_f32 v15, v15, v88\n"
        "v_mov_b32 v88, 0x3727c5ac\n"
        "v_fmac_f32 v88, 0x3b000000, v15\n"
        "v_rsq_f32 v15, v88\n"
        "s_nop 0\n"
        "v_mul_f32 v118, 0x3e0293ee, v15\n"
        "v_mov_b32 v119, 1\n"
        "s_lshl_b32 s42, s29, 2\n"
        "v_mov_b32 v56, s42\n"
        "v_mov_b32 v58, 1\n"
        "s_lshl_b32 s42, s31, 11\n"
        "s_lshl_b32 s43, s30, 8\n"
        "s_add_u32 s42, s42, s43\n"
        "s_lshl_b32 s43, s29, 6\n"
        "s_add_u32 s42, s42, s43\n"
        "s_add_u32 s42, s42, 266240\n"
        "v_add_u32 v117, s42, v3\n"
        "v_mov_b32 v122, s38\n"
        "v_mov_b32 v123, s39\n"
        "s_mov_b32 s52, 0x00010001\n"
        "s_mov_b32 s53, 0x00010001\n"
        "v_lshrrev_b32 v46, 1, v1\n"
        "v_and_b32 v46, 8, v46\n"
        "v_lshl_or_b32 v46, v5, 1, v46\n"
        "v_lshrrev_b32 v47, 5, v1\n"
        "v_or_b32 v46, v46, v47\n"
        "v_lshlrev_b32 v47, 3, v6\n"
        "v_lshlrev_b32 v45, 4, v46\n"
        "v_xor_b32 v47, v47, v45\n"
        "v_lshl_or_b32 v47, v46, 10, v47\n"
        "s_lshl_b32 s42, s29, 8\n"
        "s_add_u32 s42, s42, 32768\n"
        "v_add_u32 v24, s42, v47\n"
        "v_xor_b32 v25, 32, v24\n"
        "v_xor_b32 v26, 64, v24\n"
        "v_xor_b32 v27, 96, v24\n"
        "v_xor_b32 v28, 128, v24\n"
        "v_xor_b32 v29, 160, v24\n"
        "v_xor_b32 v30, 192, v24\n"
        "v_xor_b32 v31, 224, v24\n"
        "s_sleep 20\n"
        "s_mov_b32 s41, 0\n"
        "H1_LOOP:\n"
        "global_load_dwordx4 v[88:91], v32, s[50:51] offset:-2048 sc1\n"
        "global_load_dwordx4 v[92:95], v32, s[50:51] offset:2048 sc1\n"
        "s_waitcnt vmcnt(0)\n"
        "v_bitop3_b32 v114, v89, v91, s38 bitop3:0x7e\n"
        "v_bitop3_b32 v115, v93, v95, s38 bitop3:0x7e\n"
        "v_or_b32 v114, v114, v115\n"
        "v_add_f32 v112, v88, v92\n"
        "v_add_f32 v113, v90, v94\n"
        "v_cmp_ne_u32 vcc, 0, v114\n"
        "s_cbranch_vccz H1_DONE\n"
        "s_add_u32 s41, s41, 1\n"
        "s_cmp_lt_u32 s41, 32768\n"
        "s_cbranch_scc1 H1_LOOP\n"
        "H1_DONE:\n"
        "v_permlane32_swap_b32 v112, v113\n"
        "v_add_f32 v112, v112, v113\n"
        "v_mov_b32 v113, v112\n"
        "s_nop 1\n"
        "v_permlane16_swap_b32 v112, v113\n"
        "v_add_f32 v112, v112, v113\n"
        "v_cvt_pk_bf16_f32 v112, v112, v112\n"
        "ds_write_b16 v33, v112 offset:512\n"
        "ds_write_b32 v56, v58 offset:16\n"
        "s_mov_b32 s41, 0\n"
        "QX_LOOP:\n"
        "ds_read_b128 v[64:67], v12 offset:16\n"
        "ds_read_b128 v[72:75], v3 offset:512\n"
        "ds_read_b128 v[76:79], v3 offset:576\n"
        "ds_read_b128 v[80:83], v3 offset:640\n"
        "ds_read_b128 v[84:87], v3 offset:704\n"
        "s_add_u32 s41, s41, 1\n"
        "s_cmp_gt_u32 s41, 524288\n"
        "s_waitcnt lgkmcnt(4)\n"
        "v_add3_u32 v64, v64, v65, v66\n"
        "v_add_u32 v64, v64, v67\n"
        "v_cmp_ne_u32 vcc, 4, v64\n"
        "s_cbranch_scc1 QX_DONE\n"
        "s_cbranch_vccnz QX_LOOP\n"
        "QX_DONE:\n"
        "s_waitcnt lgkmcnt(0)\n"
        "v_mfma_f32_16x16x32_bf16 v[68:71], v[128:131], v[72:75], 0\n"
        "v_mfma_f32_16x16x32_bf16 v[68:71], v[132:135], v[76:79], v[68:71]\n"
        "v_mfma_f32_16x16x32_bf16 v[68:71], v[136:139], v[80:83], v[68:71]\n"
        "v_mfma_f32_16x16x32_bf16 v[68:71], v[140:143], v[84:87], v[68:71]\n"
        "s_mov_b64 exec, s[52:53]\n"
        "s_nop 6\n"
        "v_pk_mul_f32 v[68:69], v[68:69], v[118:119] op_sel_hi:[1,0]\n"
        "v_pk_mul_f32 v[70:71], v[70:71], v[118:119] op_sel_hi:[1,0]\n"
        "v_cvt_pk_bf16_f32 v120, v68, v69\n"
        "v_cvt_pk_bf16_f32 v121, v70, v71\n"
        "global_store_dwordx4 v117, v[120:123], s[4:5] sc1\n"
        "s_mov_b64 exec, -1\n"
        "s_mov_b32 s41, 0\n"
        "LAND0:\n"
        "ds_read_b128 v[64:67], v12 offset:32\n"
        "s_add_u32 s41, s41, 1\n"
        "s_cmp_gt_u32 s41, 524288\n"
        "s_waitcnt lgkmcnt(0)\n"
        "v_add3_u32 v64, v64, v65, v66\n"
        "v_add_u32 v64, v64, v67\n"
        "v_cmp_ne_u32 vcc, 4, v64\n"
        "s_cbranch_scc1 LAND0_D\n"
        "s_cbranch_vccz LAND0_D\n"
        "s_sleep 2\n"
        "s_branch LAND0\n"
        "LAND0_D:\n"
        "ds_read_b64_tr_b16 v[128:129], v24 offset:0\n"
        "ds_read_b64_tr_b16 v[130:131], v24 offset:16384\n"
        "ds_read_b64_tr_b16 v[132:133], v25 offset:0\n"
        "ds_read_b64_tr_b16 v[134:135], v25 offset:16384\n"
        "ds_read_b64_tr_b16 v[136:137], v26 offset:0\n"
        "ds_read_b64_tr_b16 v[138:139], v26 offset:16384\n"
        "ds_read_b64_tr_b16 v[140:141], v27 offset:0\n"
        "ds_read_b64_tr_b16 v[142:143], v27 offset:16384\n"
        "ds_read_b64_tr_b16 v[144:145], v28 offset:0\n"
        "ds_read_b64_tr_b16 v[146:147], v28 offset:16384\n"
        "ds_read_b64_tr_b16 v[148:149], v29 offset:0\n"
        "ds_read_b64_tr_b16 v[150:151], v29 offset:16384\n"
        "ds_read_b64_tr_b16 v[152:153], v30 offset:0\n"
        "ds_read_b64_tr_b16 v[154:155], v30 offset:16384\n"
        "ds_read_b64_tr_b16 v[156:157], v31 offset:0\n"
        "ds_read_b64_tr_b16 v[158:159], v31 offset:16384\n"
        "ds_read_b64_tr_b16 v[160:161], v24 offset:32768\n"
        "ds_read_b64_tr_b16 v[162:163], v24 offset:49152\n"
        "ds_read_b64_tr_b16 v[164:165], v25 offset:32768\n"
        "ds_read_b64_tr_b16 v[166:167], v25 offset:49152\n"
        "ds_read_b64_tr_b16 v[168:169], v26 offset:32768\n"
        "ds_read_b64_tr_b16 v[170:171], v26 offset:49152\n"
        "ds_read_b64_tr_b16 v[172:173], v27 offset:32768\n"
        "ds_read_b64_tr_b16 v[174:175], v27 offset:49152\n"
        "ds_read_b64_tr_b16 v[176:177], v28 offset:32768\n"
        "ds_read_b64_tr_b16 v[178:179], v28 offset:49152\n"
        "ds_read_b64_tr_b16 v[180:181], v29 offset:32768\n"
        "ds_read_b64_tr_b16 v[182:183], v29 offset:49152\n"
        "ds_read_b64_tr_b16 v[184:185], v30 offset:32768\n"
        "ds_read_b64_tr_b16 v[186:187], v30 offset:49152\n"
        "ds_read_b64_tr_b16 v[188:189], v31 offset:32768\n"
        "ds_read_b64_tr_b16 v[190:191], v31 offset:49152\n"
        "v_add_u32 v24, 0x10000, v24\n"
        "v_add_u32 v25, 0x10000, v25\n"
        "v_add_u32 v26, 0x10000, v26\n"
        "v_add_u32 v27, 0x10000, v27\n"
        "v_add_u32 v28, 0x10000, v28\n"
        "v_add_u32 v29, 0x10000, v29\n"
        "v_add_u32 v30, 0x10000, v30\n"
        "v_add_u32 v31, 0x10000, v31\n"
        "s_lshl_b32 s42, s35, 15\n"
        "s_lshl_b32 s43, s29, 13\n"
        "s_add_u32 s42, s42, s43\n"
        "s_add_u32 s42, s42, 270336\n"
        "v_add_u32 v124, s42, v2\n"
        "s_waitcnt lgkmcnt(0)\n"
        "ds_write_b32 v56, v58 offset:64\n"
        "v_lshlrev_b32 v46, 7, v3\n"
        "v_lshrrev_b32 v47, 1, v6\n"
        "v_lshl_or_b32 v46, v47, 10, v46\n"
        "v_lshl_or_b32 v46, v5, 8, v46\n"
        "v_and_b32 v47, 1, v6\n"
        "v_lshl_or_b32 v46, v47, 3, v46\n"
        "s_lshl_b32 s42, s29, 6\n"
        "v_or_b32 v125, s42, v46\n"
        "s_lshl_b32 s42, s35, 7\n"
        "s_add_u32 s42, s42, s36\n"
        "s_mul_i32 s42, s42, 18688\n"
        "s_add_u32 s42, s42, 987136\n"
        "s_add_u32 s48, s4, s42\n"
        "s_addc_u32 s49, s5, 0\n"
        "v_lshl_or_b32 v46, v4, 6, v3\n"
        "v_or_b32 v53, s46, v46\n"
        "v_and_b32 v46, 32, v1\n"
        "v_lshlrev_b32 v46, 3, v46\n"
        "v_lshl_or_b32 v46, v4, 4, v46\n"
        "s_lshl_b32 s42, s29, 9\n"
        "s_add_u32 s42, s42, 16384\n"
        "v_add_u32 v54, s42, v46\n"
        "s_lshl_b32 s42, s35, 7\n"
        "s_lshl_b32 s43, s28, 4\n"
        "s_add_u32 s42, s42, s43\n"
        "v_lshrrev_b32 v46, 3, v1\n"
        "v_add_u32 v46, s42, v46\n"
        "v_mul_u32_u24 v46, 18688, v46\n"
        "s_lshl_b32 s42, s30, 8\n"
        "s_lshl_b32 s43, s34, 4\n"
        "s_add_u32 s42, s42, s43\n"
        "s_add_u32 s42, s42, 1003520\n"
        "v_add_u32 v35, s42, v46\n"
        "s_lshl_b32 s42, s30, 11\n"
        "s_lshl_b32 s43, s34, 6\n"
        "s_add_u32 s42, s42, s43\n"
        "s_add_u32 s42, s42, 987136\n"
        "v_and_b32 v47, 4, v1\n"
        "v_lshlrev_b32 v47, 8, v47\n"
        "v_lshl_or_b32 v47, v6, 4, v47\n"
        "v_add3_u32 v34, v46, v47, s42\n"
        "s_add_u32 s72, s4, 149504\n"
        "s_addc_u32 s73, s5, 0\n"
        "v_lshrrev_b32 v46, 5, v1\n"
        "s_lshl_b32 s42, s28, 1\n"
        "v_add_u32 v46, s42, v46\n"
        "v_lshlrev_b32 v37, 2, v46\n"
        "v_mul_u32_u24 v46, 144, v46\n"
        "v_and_b32 v47, 4, v1\n"
        "v_lshlrev_b32 v47, 4, v47\n"
        "v_lshl_or_b32 v47, v6, 3, v47\n"
        "v_and_b32 v39, 16, v1\n"
        "v_lshl_or_b32 v47, v39, 1, v47\n"
        "v_add_u32 v36, v46, v47\n"
        "v_cmp_eq_u32 vcc, 0, v39\n"
        "v_mov_b32 v46, 7360\n"
        "s_nop 0\n"
        "v_cndmask_b32 v37, v46, v37, vcc\n"
        "s_lshl_b32 s42, s28, 2\n"
        "v_mov_b32 v38, s42\n"
        "v_and_b32 v46, 1, v1\n"
        "v_lshlrev_b32 v46, 4, v46\n"
        "v_mov_b32 v47, 0xffff\n"
        "v_lshlrev_b32 v46, v46, v47\n"
        "v_bfe_u32 v47, v1, 1, 2\n"
        "v_cmp_eq_u32 vcc, 0, v47\n"
        "s_nop 1\n"
        "v_cndmask_b32 v40, 0, v46, vcc\n"
        "v_cmp_eq_u32 vcc, 1, v47\n"
        "s_nop 1\n"
        "v_cndmask_b32 v41, 0, v46, vcc\n"
        "v_cmp_eq_u32 vcc, 2, v47\n"
        "s_nop 1\n"
        "v_cndmask_b32 v42, 0, v46, vcc\n"
        "v_cmp_eq_u32 vcc, 3, v47\n"
        "s_nop 1\n"
        "v_cndmask_b32 v43, 0, v46, vcc\n"
        "s_lshl_b32 s42, s36, 18\n"
        "s_lshl_b32 s47, s29, 14\n"
        "s_add_u32 s42, s42, s47\n"
        "s_add_u32 s50, s8, s42\n"
        "s_addc_u32 s51, s9, 0\n"
        "s_add_u32 s47, s47, 32768\n"
        "s_lshr_b32 s37, s74, 6\n"
        "s_lshl_b32 s42, s30, 2\n"
        "s_add_u32 s37, s37, s42\n"
        "s_bfe_u32 s40, s74, 0x30003\n"
        "s_lshl_b32 s42, s37, 17\n"
        "s_lshl_b32 s43, s29, 15\n"
        "s_add_u32 s42, s42, s43\n"
        "s_lshl_b32 s43, s40, 7\n"
        "s_add_u32 s42, s42, s43\n"
        "s_add_u32 s82, s20, s42\n"
        "s_addc_u32 s83, s21, 0\n"
        "v_lshrrev_b32 v46, 3, v1\n"
        "v_and_b32 v47, 7, v1\n"
        "v_bitop3_b32 v47, v46, v47, 2 bitop3:0x6c\n"
        "v_lshlrev_b32 v47, 4, v47\n"
        "v_lshl_or_b32 v48, v46, 10, v47\n"
        "v_xor_b32 v52, 64, v48\n"
        "v_add_u32 v49, 0x2000, v52\n"
        "v_add_u32 v50, 0x4000, v48\n"
        "v_add_u32 v51, 0x6000, v52\n"
        "s_sleep 16\n"
        "global_load_dwordx4 v[88:91], v124, s[4:5] offset:-4096 sc1\n"
        "global_load_dwordx4 v[92:95], v124, s[4:5] offset:-3072 sc1\n"
        "global_load_dwordx4 v[96:99], v124, s[4:5] offset:-2048 sc1\n"
        "global_load_dwordx4 v[100:103], v124, s[4:5] offset:-1024 sc1\n"
        "global_load_dwordx4 v[104:107], v124, s[4:5] offset:0 sc1\n"
        "global_load_dwordx4 v[108:111], v124, s[4:5] offset:1024 sc1\n"
        "global_load_dwordx4 v[112:115], v124, s[4:5] offset:2048 sc1\n"
        "global_load_dwordx4 v[116:119], v124, s[4:5] offset:3072 sc1\n"
        "s_mov_b32 s41, 0\n"
        "LAND1:\n"
        "ds_read_b128 v[64:67], v12 offset:48\n"
        "s_add_u32 s41, s41, 1\n"
        "s_cmp_gt_u32 s41, 524288\n"
        "s_waitcnt lgkmcnt(0)\n"
        "v_add3_u32 v64, v64, v65, v66\n"
        "v_add_u32 v64, v64, v67\n"
        "v_cmp_ne_u32 vcc, 4, v64\n"
        "s_cbranch_scc1 LAND1_D\n"
        "s_cbranch_vccz LAND1_D\n"
        "s_sleep 2\n"
        "s_branch LAND1\n"
        "LAND1_D:\n"
        "ds_read_b64_tr_b16 v[192:193], v24 offset:0\n"
        "ds_read_b64_tr_b16 v[194:195], v24 offset:16384\n"
        "ds_read_b64_tr_b16 v[196:197], v25 offset:0\n"
        "ds_read_b64_tr_b16 v[198:199], v25 offset:16384\n"
        "ds_read_b64_tr_b16 v[200:201], v26 offset:0\n"
        "ds_read_b64_tr_b16 v[202:203], v26 offset:16384\n"
        "ds_read_b64_tr_b16 v[204:205], v27 offset:0\n"
        "ds_read_b64_tr_b16 v[206:207], v27 offset:16384\n"
        "ds_read_b64_tr_b16 v[208:209], v28 offset:0\n"
        "ds_read_b64_tr_b16 v[210:211], v28 offset:16384\n"
        "ds_read_b64_tr_b16 v[212:213], v29 offset:0\n"
        "ds_read_b64_tr_b16 v[214:215], v29 offset:16384\n"
        "ds_read_b64_tr_b16 v[216:217], v30 offset:0\n"
        "ds_read_b64_tr_b16 v[218:219], v30 offset:16384\n"
        "ds_read_b64_tr_b16 v[220:221], v31 offset:0\n"
        "ds_read_b64_tr_b16 v[222:223], v31 offset:16384\n"
        "ds_read_b64_tr_b16 v[224:225], v24 offset:32768\n"
        "ds_read_b64_tr_b16 v[226:227], v24 offset:49152\n"
        "ds_read_b64_tr_b16 v[228:229], v25 offset:32768\n"
        "ds_read_b64_tr_b16 v[230:231], v25 offset:49152\n"
        "ds_read_b64_tr_b16 v[232:233], v26 offset:32768\n"
        "ds_read_b64_tr_b16 v[234:235], v26 offset:49152\n"
        "ds_read_b64_tr_b16 v[236:237], v27 offset:32768\n"
        "ds_read_b64_tr_b16 v[238:239], v27 offset:49152\n"
        "ds_read_b64_tr_b16 v[240:241], v28 offset:32768\n"
        "ds_read_b64_tr_b16 v[242:243], v28 offset:49152\n"
        "ds_read_b64_tr_b16 v[244:245], v29 offset:32768\n"
        "ds_read_b64_tr_b16 v[246:247], v29 offset:49152\n"
        "ds_read_b64_tr_b16 v[248:249], v30 offset:32768\n"
        "ds_read_b64_tr_b16 v[250:251], v30 offset:49152\n"
        "ds_read_b64_tr_b16 v[252:253], v31 offset:32768\n"
        "ds_read_b64_tr_b16 v[254:255], v31 offset:49152\n"
        "v_add_u32 v24, 0xffff0000, v24\n"
        "v_add_u32 v25, 0xffff0000, v25\n"
        "v_add_u32 v26, 0xffff0000, v26\n"
        "v_add_u32 v27, 0xffff0000, v27\n"
        "v_add_u32 v28, 0xffff0000, v28\n"
        "v_add_u32 v29, 0xffff0000, v29\n"
        "v_add_u32 v30, 0xffff0000, v30\n"
        "v_add_u32 v31, 0xffff0000, v31\n"
        "s_waitcnt lgkmcnt(0)\n"
        "ds_write_b32 v56, v58 offset:80\n"
        "s_mov_b32 s41, 0\n"
        "H2_LOOP:\n"
        "s_waitcnt vmcnt(0)\n"
        "v_cmp_ne_u64 s[52:53], v[90:91], s[38:39]\n"
        "v_cmp_ne_u64 s[54:55], v[94:95], s[38:39]\n"
        "v_cmp_ne_u64 s[56:57], v[98:99], s[38:39]\n"
        "v_cmp_ne_u64 s[58:59], v[102:103], s[38:39]\n"
        "v_cmp_ne_u64 s[60:61], v[106:107], s[38:39]\n"
        "v_cmp_ne_u64 s[62:63], v[110:111], s[38:39]\n"
        "v_cmp_ne_u64 s[64:65], v[114:115], s[38:39]\n"
        "v_cmp_ne_u64 s[66:67], v[118:119], s[38:39]\n"
        "s_or_b64 s[26:27], s[52:53], s[54:55]\n"
        "s_or_b64 s[26:27], s[26:27], s[56:57]\n"
        "s_or_b64 s[26:27], s[26:27], s[58:59]\n"
        "s_or_b64 s[26:27], s[26:27], s[60:61]\n"
        "s_or_b64 s[26:27], s[26:27], s[62:63]\n"
        "s_or_b64 s[26:27], s[26:27], s[64:65]\n"
        "s_or_b64 s[26:27], s[26:27], s[66:67]\n"
        "s_cbranch_scc0 H2_DONE\n"
        "s_mov_b64 exec, s[52:53]\n"
        "global_load_dwordx4 v[88:91], v124, s[4:5] offset:-4096 sc1\n"
        "s_mov_b64 exec, s[54:55]\n"
        "global_load_dwordx4 v[92:95], v124, s[4:5] offset:-3072 sc1\n"
        "s_mov_b64 exec, s[56:57]\n"
        "global_load_dwordx4 v[96:99], v124, s[4:5] offset:-2048 sc1\n"
        "s_mov_b64 exec, s[58:59]\n"
        "global_load_dwordx4 v[100:103], v124, s[4:5] offset:-1024 sc1\n"
        "s_mov_b64 exec, s[60:61]\n"
        "global_load_dwordx4 v[104:107], v124, s[4:5] offset:0 sc1\n"
        "s_mov_b64 exec, s[62:63]\n"
        "global_load_dwordx4 v[108:111], v124, s[4:5] offset:1024 sc1\n"
        "s_mov_b64 exec, s[64:65]\n"
        "global_load_dwordx4 v[112:115], v124, s[4:5] offset:2048 sc1\n"
        "s_mov_b64 exec, s[66:67]\n"
        "global_load_dwordx4 v[116:119], v124, s[4:5] offset:3072 sc1\n"
        "s_mov_b64 exec, -1\n"
        "s_add_u32 s41, s41, 1\n"
        "s_cmp_lt_u32 s41, 32768\n"
        "s_cbranch_scc1 H2_LOOP\n"
        "s_waitcnt vmcnt(0)\n"
        "H2_DONE:\n"
        "ds_write_b64 v125, v[88:89] offset:16384\n"
        "ds_write_b64 v125, v[92:93] offset:24576\n"
        "ds_write_b64 v125, v[96:97] offset:16400\n"
        "ds_write_b64 v125, v[100:101] offset:24592\n"
        "ds_write_b64 v125, v[104:105] offset:16416\n"
        "ds_write_b64 v125, v[108:109] offset:24608\n"
        "ds_write_b64 v125, v[112:113] offset:16432\n"
        "ds_write_b64 v125, v[116:117] offset:24624\n"
        "s_waitcnt lgkmcnt(0)\n"
        "s_barrier\n"
        "s_add_u32 m0, s47, 69632\n"
        "v_xor_b32 v62, 0x31040, v2\n"
        "global_load_lds_dwordx4 v62, s[50:51] offset:0\n"
        "v_xor_b32 v62, 0x31050, v2\n"
        "global_load_lds_dwordx4 v62, s[50:51] offset:1024\n"
        "v_xor_b32 v62, 0x31060, v2\n"
        "global_load_lds_dwordx4 v62, s[50:51] offset:2048\n"
        "v_xor_b32 v62, 0x31070, v2\n"
        "global_load_lds_dwordx4 v62, s[50:51] offset:3072\n"
        "s_add_u32 m0, s47, 73728\n"
        "v_xor_b32 v62, 0x32080, v2\n"
        "global_load_lds_dwordx4 v62, s[50:51] offset:0\n"
        "v_xor_b32 v62, 0x32090, v2\n"
        "global_load_lds_dwordx4 v62, s[50:51] offset:1024\n"
        "v_xor_b32 v62, 0x320a0, v2\n"
        "global_load_lds_dwordx4 v62, s[50:51] offset:2048\n"
        "v_xor_b32 v62, 0x320b0, v2\n"
        "global_load_lds_dwordx4 v62, s[50:51] offset:3072\n"
        "v_lshlrev_b32 v60, 4, v4\n"
        "s_barrier\n"
        "ds_read_b128 v[64:67], v60 offset:256\n"
        "s_add_u32 m0, s47, 77824\n"
        "v_xor_b32 v62, 0x330c0, v2\n"
        "global_load_lds_dwordx4 v62, s[50:51] offset:0\n"
        "v_xor_b32 v62, 0x330d0, v2\n"
        "global_load_lds_dwordx4 v62, s[50:51] offset:1024\n"
        "v_xor_b32 v62, 0x330e0, v2\n"
        "global_load_lds_dwordx4 v62, s[50:51] offset:2048\n"
        "v_xor_b32 v62, 0x330f0, v2\n"
        "global_load_lds_dwordx4 v62, s[50:51] offset:3072\n"
        "s_waitcnt lgkmcnt(0)\n"
        "v_max3_f32 v64, v64, v65, v66\n"
        "v_max_f32 v55, v64, v67\n"
        "s_barrier\n"
        "ds_read_b128 v[72:75], v2 offset:4096\n"
        "ds_read_b128 v[76:79], v2 offset:5120\n"
        "ds_read_b128 v[80:83], v2 offset:6144\n"
        "ds_read_b128 v[84:87], v2 offset:7168\n"
        "ds_read_b128 v[64:67], v2 offset:1536\n"
        "ds_read_b128 v[68:71], v2 offset:2560\n"
        "s_waitcnt lgkmcnt(5)\n"
        "v_mfma_f32_16x16x32_bf16 v[120:123], v[16:19], v[72:75], 0\n"
        "v_mfma_f32_16x16x32_bf16 v[88:91], v[128:131], v[72:75], 0\n"
        "v_mfma_f32_16x16x32_bf16 v[92:95], v[132:135], v[72:75], 0\n"
        "v_mfma_f32_16x16x32_bf16 v[96:99], v[136:139], v[72:75], 0\n"
        "v_mfma_f32_16x16x32_bf16 v[100:103], v[140:143], v[72:75], 0\n"
        "v_mfma_f32_16x16x32_bf16 v[104:107], v[144:147], v[72:75], 0\n"
        "v_mfma_f32_16x16x32_bf16 v[108:111], v[148:151], v[72:75], 0\n"
        "v_mfma_f32_16x16x32_bf16 v[112:115], v[152:155], v[72:75], 0\n"
        "v_mfma_f32_16x16x32_bf16 v[116:119], v[156:159], v[72:75], 0\n"
        "s_waitcnt lgkmcnt(0)\n"
        "v_mfma_f32_16x16x32_bf16 v[120:123], v[16:19], v[76:79], v[120:123]\n"
        "v_mfma_f32_16x16x32_bf16 v[88:91], v[160:163], v[76:79], v[88:91]\n"
        "ds_read_b64_tr_b16 v[128:129], v24 offset:0\n"
        "ds_read_b64_tr_b16 v[130:131], v24 offset:16384\n"
        "v_mfma_f32_16x16x32_bf16 v[92:95], v[164:167], v[76:79], v[92:95]\n"
        "ds_read_b64_tr_b16 v[132:133], v25 offset:0\n"
        "ds_read_b64_tr_b16 v[134:135], v25 offset:16384\n"
        "v_mfma_f32_16x16x32_bf16 v[96:99], v[168:171], v[76:79], v[96:99]\n"
        "ds_read_b64_tr_b16 v[136:137], v26 offset:0\n"
        "ds_read_b64_tr_b16 v[138:139], v26 offset:16384\n"
        "v_mfma_f32_16x16x32_bf16 v[100:103], v[172:175], v[76:79], v[100:103]\n"
        "ds_read_b64_tr_b16 v[140:141], v27 offset:0\n"
        "ds_read_b64_tr_b16 v[142:143], v27 offset:16384\n"
        "v_mfma_f32_16x16x32_bf16 v[104:107], v[176:179], v[76:79], v[104:107]\n"
        "ds_read_b64_tr_b16 v[144:145], v28 offset:0\n"
        "ds_read_b64_tr_b16 v[146:147], v28 offset:16384\n"
        "v_mfma_f32_16x16x32_bf16 v[108:111], v[180:183], v[76:79], v[108:111]\n"
        "ds_read_b64_tr_b16 v[148:149], v29 offset:0\n"
        "ds_read_b64_tr_b16 v[150:151], v29 offset:16384\n"
        "v_mfma_f32_16x16x32_bf16 v[112:115], v[184:187], v[76:79], v[112:115]\n"
        "ds_read_b64_tr_b16 v[152:153], v30 offset:0\n"
        "ds_read_b64_tr_b16 v[154:155], v30 offset:16384\n"
        "v_mfma_f32_16x16x32_bf16 v[116:119], v[188:191], v[76:79], v[116:119]\n"
        "ds_read_b64_tr_b16 v[156:157], v31 offset:0\n"
        "ds_read_b64_tr_b16 v[158:159], v31 offset:16384\n"
        "v_mfma_f32_16x16x32_bf16 v[120:123], v[16:19], v[80:83], v[120:123]\n"
        "v_mfma_f32_16x16x32_bf16 v[88:91], v[192:195], v[80:83], v[88:91]\n"
        "ds_read_b64_tr_b16 v[160:161], v24 offset:32768\n"
        "ds_read_b64_tr_b16 v[162:163], v24 offset:49152\n"
        "v_mfma_f32_16x16x32_bf16 v[92:95], v[196:199], v[80:83], v[92:95]\n"
        "ds_read_b64_tr_b16 v[164:165], v25 offset:32768\n"
        "ds_read_b64_tr_b16 v[166:167], v25 offset:49152\n"
        "v_mfma_f32_16x16x32_bf16 v[96:99], v[200:203], v[80:83], v[96:99]\n"
        "ds_read_b64_tr_b16 v[168:169], v26 offset:32768\n"
        "ds_read_b64_tr_b16 v[170:171], v26 offset:49152\n"
        "v_mfma_f32_16x16x32_bf16 v[100:103], v[204:207], v[80:83], v[100:103]\n"
        "ds_read_b64_tr_b16 v[172:173], v27 offset:32768\n"
        "ds_read_b64_tr_b16 v[174:175], v27 offset:49152\n"
        "v_mfma_f32_16x16x32_bf16 v[104:107], v[208:211], v[80:83], v[104:107]\n"
        "ds_read_b64_tr_b16 v[176:177], v28 offset:32768\n"
        "ds_read_b64_tr_b16 v[178:179], v28 offset:49152\n"
        "v_mfma_f32_16x16x32_bf16 v[108:111], v[212:215], v[80:83], v[108:111]\n"
        "ds_read_b64_tr_b16 v[180:181], v29 offset:32768\n"
        "ds_read_b64_tr_b16 v[182:183], v29 offset:49152\n"
        "v_mfma_f32_16x16x32_bf16 v[112:115], v[216:219], v[80:83], v[112:115]\n"
        "ds_read_b64_tr_b16 v[184:185], v30 offset:32768\n"
        "ds_read_b64_tr_b16 v[186:187], v30 offset:49152\n"
        "v_mfma_f32_16x16x32_bf16 v[116:119], v[220:223], v[80:83], v[116:119]\n"
        "ds_read_b64_tr_b16 v[188:189], v31 offset:32768\n"
        "ds_read_b64_tr_b16 v[190:191], v31 offset:49152\n"
        "v_mfma_f32_16x16x32_bf16 v[120:123], v[16:19], v[84:87], v[120:123]\n"
        "v_mfma_f32_16x16x32_bf16 v[88:91], v[224:227], v[84:87], v[88:91]\n"
        "v_mfma_f32_16x16x32_bf16 v[92:95], v[228:231], v[84:87], v[92:95]\n"
        "v_mfma_f32_16x16x32_bf16 v[96:99], v[232:235], v[84:87], v[96:99]\n"
        "v_mfma_f32_16x16x32_bf16 v[100:103], v[236:239], v[84:87], v[100:103]\n"
        "v_mfma_f32_16x16x32_bf16 v[104:107], v[240:243], v[84:87], v[104:107]\n"
        "v_mfma_f32_16x16x32_bf16 v[108:111], v[244:247], v[84:87], v[108:111]\n"
        "v_mfma_f32_16x16x32_bf16 v[112:115], v[248:251], v[84:87], v[112:115]\n"
        "v_mfma_f32_16x16x32_bf16 v[116:119], v[252:255], v[84:87], v[116:119]\n"
        "s_waitcnt lgkmcnt(0)\n"
        "v_mfma_f32_16x16x32_bf16 v[120:123], v[16:19], v[64:67], v[120:123]\n"
        "v_mfma_f32_16x16x32_bf16 v[88:91], v[128:131], v[64:67], v[88:91]\n"
        "v_mfma_f32_16x16x32_bf16 v[92:95], v[132:135], v[64:67], v[92:95]\n"
        "v_mfma_f32_16x16x32_bf16 v[96:99], v[136:139], v[64:67], v[96:99]\n"
        "v_mfma_f32_16x16x32_bf16 v[100:103], v[140:143], v[64:67], v[100:103]\n"
        "v_mfma_f32_16x16x32_bf16 v[104:107], v[144:147], v[64:67], v[104:107]\n"
        "v_mfma_f32_16x16x32_bf16 v[108:111], v[148:151], v[64:67], v[108:111]\n"
        "v_mfma_f32_16x16x32_bf16 v[112:115], v[152:155], v[64:67], v[112:115]\n"
        "v_mfma_f32_16x16x32_bf16 v[116:119], v[156:159], v[64:67], v[116:119]\n"
        "v_mfma_f32_16x16x32_bf16 v[120:123], v[16:19], v[68:71], v[120:123]\n"
        "v_mfma_f32_16x16x32_bf16 v[88:91], v[160:163], v[68:71], v[88:91]\n"
        "v_mfma_f32_16x16x32_bf16 v[92:95], v[164:167], v[68:71], v[92:95]\n"
        "v_mfma_f32_16x16x32_bf16 v[96:99], v[168:171], v[68:71], v[96:99]\n"
        "v_mfma_f32_16x16x32_bf16 v[100:103], v[172:175], v[68:71], v[100:103]\n"
        "v_mfma_f32_16x16x32_bf16 v[104:107], v[176:179], v[68:71], v[104:107]\n"
        "v_mfma_f32_16x16x32_bf16 v[108:111], v[180:183], v[68:71], v[108:111]\n"
        "v_mfma_f32_16x16x32_bf16 v[112:115], v[184:187], v[68:71], v[112:115]\n"
        "v_mfma_f32_16x16x32_bf16 v[116:119], v[188:191], v[68:71], v[116:119]\n"
        "v_add_u32 v24, 0x10000, v24\n"
        "v_add_u32 v25, 0x10000, v25\n"
        "v_add_u32 v26, 0x10000, v26\n"
        "v_add_u32 v27, 0x10000, v27\n"
        "v_add_u32 v28, 0x10000, v28\n"
        "v_add_u32 v29, 0x10000, v29\n"
        "v_add_u32 v30, 0x10000, v30\n"
        "v_add_u32 v31, 0x10000, v31\n"
        "s_waitcnt vmcnt(0)\n"
        "s_barrier\n"
        "ds_read_b64_tr_b16 v[192:193], v24 offset:0\n"
        "ds_read_b64_tr_b16 v[194:195], v24 offset:16384\n"
        "ds_read_b64_tr_b16 v[196:197], v25 offset:0\n"
        "ds_read_b64_tr_b16 v[198:199], v25 offset:16384\n"
        "ds_read_b64_tr_b16 v[200:201], v26 offset:0\n"
        "ds_read_b64_tr_b16 v[202:203], v26 offset:16384\n"
        "ds_read_b64_tr_b16 v[204:205], v27 offset:0\n"
        "ds_read_b64_tr_b16 v[206:207], v27 offset:16384\n"
        "ds_read_b64_tr_b16 v[208:209], v28 offset:0\n"
        "ds_read_b64_tr_b16 v[210:211], v28 offset:16384\n"
        "ds_read_b64_tr_b16 v[212:213], v29 offset:0\n"
        "ds_read_b64_tr_b16 v[214:215], v29 offset:16384\n"
        "ds_read_b64_tr_b16 v[216:217], v30 offset:0\n"
        "ds_read_b64_tr_b16 v[218:219], v30 offset:16384\n"
        "ds_read_b64_tr_b16 v[220:221], v31 offset:0\n"
        "ds_read_b64_tr_b16 v[222:223], v31 offset:16384\n"
        "ds_read_b64_tr_b16 v[224:225], v24 offset:32768\n"
        "ds_read_b64_tr_b16 v[226:227], v24 offset:49152\n"
        "ds_read_b64_tr_b16 v[228:229], v25 offset:32768\n"
        "ds_read_b64_tr_b16 v[230:231], v25 offset:49152\n"
        "ds_read_b64_tr_b16 v[232:233], v26 offset:32768\n"
        "ds_read_b64_tr_b16 v[234:235], v26 offset:49152\n"
        "ds_read_b64_tr_b16 v[236:237], v27 offset:32768\n"
        "ds_read_b64_tr_b16 v[238:239], v27 offset:49152\n"
        "ds_read_b64_tr_b16 v[240:241], v28 offset:32768\n"
        "ds_read_b64_tr_b16 v[242:243], v28 offset:49152\n"
        "ds_read_b64_tr_b16 v[244:245], v29 offset:32768\n"
        "ds_read_b64_tr_b16 v[246:247], v29 offset:49152\n"
        "ds_read_b64_tr_b16 v[248:249], v30 offset:32768\n"
        "ds_read_b64_tr_b16 v[250:251], v30 offset:49152\n"
        "ds_read_b64_tr_b16 v[252:253], v31 offset:32768\n"
        "ds_read_b64_tr_b16 v[254:255], v31 offset:49152\n"
        "s_mov_b32 s26, 0xffff\n"
        "s_mov_b32 s27, 0xffff\n"
        "s_barrier\n"
        "ds_read_b32 v61, v12 offset:0\n"
        "ds_read_b128 v[72:75], v2 offset:4096\n"
        "ds_read_b128 v[76:79], v2 offset:5120\n"
        "s_waitcnt lgkmcnt(2)\n"
        "v_cmp_ne_u32 vcc, 0, v61\n"
        "s_cbranch_vccnz X_SLOW\n"
        "X_PV:\n"
        "s_waitcnt lgkmcnt(0)\n"
        "v_mfma_f32_16x16x32_bf16 v[88:91], v[192:195], v[72:75], v[88:91]\n"
        "v_mfma_f32_16x16x32_bf16 v[88:91], v[224:227], v[76:79], v[88:91]\n"
        "v_mfma_f32_16x16x32_bf16 v[92:95], v[196:199], v[72:75], v[92:95]\n"
        "v_mfma_f32_16x16x32_bf16 v[92:95], v[228:231], v[76:79], v[92:95]\n"
        "v_mfma_f32_16x16x32_bf16 v[120:123], v[16:19], v[72:75], v[120:123]\n"
        "v_mfma_f32_16x16x32_bf16 v[120:123], v[16:19], v[76:79], v[120:123]\n"
        "v_mfma_f32_16x16x32_bf16 v[96:99], v[200:203], v[72:75], v[96:99]\n"
        "v_mfma_f32_16x16x32_bf16 v[96:99], v[232:235], v[76:79], v[96:99]\n"
        "v_mfma_f32_16x16x32_bf16 v[100:103], v[204:207], v[72:75], v[100:103]\n"
        "v_mfma_f32_16x16x32_bf16 v[100:103], v[236:239], v[76:79], v[100:103]\n"
        "v_mov_b32 v67, s38\n"
        "v_mfma_f32_16x16x32_bf16 v[104:107], v[208:211], v[72:75], v[104:107]\n"
        "v_cvt_pk_bf16_f32 v128, v88, v89\n"
        "v_mfma_f32_16x16x32_bf16 v[104:107], v[240:243], v[76:79], v[104:107]\n"
        "v_cvt_pk_bf16_f32 v129, v90, v91\n"
        "v_mfma_f32_16x16x32_bf16 v[108:111], v[212:215], v[72:75], v[108:111]\n"
        "v_cvt_pk_bf16_f32 v130, v92, v93\n"
        "v_mfma_f32_16x16x32_bf16 v[108:111], v[244:247], v[76:79], v[108:111]\n"
        "v_cvt_pk_bf16_f32 v131, v94, v95\n"
        "global_store_dwordx4 v53, v[128:131], s[48:49] offset:0 sc1\n"
        "v_mfma_f32_16x16x32_bf16 v[112:115], v[216:219], v[72:75], v[112:115]\n"
        "v_cvt_pk_bf16_f32 v132, v96, v97\n"
        "v_mfma_f32_16x16x32_bf16 v[112:115], v[248:251], v[76:79], v[112:115]\n"
        "v_cvt_pk_bf16_f32 v133, v98, v99\n"
        "v_mfma_f32_16x16x32_bf16 v[116:119], v[220:223], v[72:75], v[116:119]\n"
        "v_cvt_pk_bf16_f32 v134, v100, v101\n"
        "v_mfma_f32_16x16x32_bf16 v[116:119], v[252:255], v[76:79], v[116:119]\n"
        "v_cvt_pk_bf16_f32 v135, v102, v103\n"
        "global_store_dwordx4 v53, v[132:135], s[48:49] offset:1024 sc1\n"
        "v_cvt_pk_bf16_f32 v136, v104, v105\n"
        "v_cvt_pk_bf16_f32 v137, v106, v107\n"
        "v_cvt_pk_bf16_f32 v138, v108, v109\n"
        "v_cvt_pk_bf16_f32 v139, v110, v111\n"
        "global_store_dwordx4 v53, v[136:139], s[48:49] offset:2048 sc1\n"
        "v_cvt_pk_bf16_f32 v64, v120, v120\n"
        "v_cvt_pk_bf16_f32 v140, v112, v113\n"
        "v_cvt_pk_bf16_f32 v141, v114, v115\n"
        "v_cvt_pk_bf16_f32 v142, v116, v117\n"
        "v_cvt_pk_bf16_f32 v143, v118, v119\n"
        "global_store_dwordx4 v53, v[140:143], s[48:49] offset:3072 sc1\n"
        "v_lshlrev_b32 v64, 16, v64\n"
        "v_sub_f32 v64, v120, v64\n"
        "v_cvt_pk_bf16_f32 v65, v120, v64\n"
        "v_mov_b32 v64, v55\n"
        "v_add3_u32 v68, v128, v129, v130\n"
        "v_add3_u32 v68, v68, v131, v132\n"
        "v_add3_u32 v68, v68, v133, v134\n"
        "v_add_u32 v68, v68, v135\n"
        "v_add3_u32 v69, v136, v137, v138\n"
        "v_add3_u32 v69, v69, v139, v140\n"
        "v_add3_u32 v69, v69, v141, v142\n"
        "v_add_u32 v69, v69, v143\n"
        "s_nop 1\n"
        "v_permlane32_swap_b32 v68, v69\n"
        "v_add_u32 v68, v68, v69\n"
        "v_mov_b32 v69, v68\n"
        "s_nop 1\n"
        "v_permlane16_swap_b32 v68, v69\n"
        "v_add_u32 v68, v68, v69\n"
        "v_add3_u32 v66, v68, v64, v65\n"
        "s_mov_b64 exec, s[26:27]\n"
        "global_store_dwordx4 v54, v[64:67], s[48:49] sc1\n"
        "s_mov_b64 exec, -1\n"
        "s_branch TAIL\n"
        "X_SLOW:\n"
        "s_waitcnt lgkmcnt(0)\n"
        "s_barrier\n"
        "ds_read_b128 v[64:67], v60 offset:256\n"
        "s_waitcnt lgkmcnt(0)\n"
        "v_max3_f32 v64, v64, v65, v66\n"
        "v_max3_f32 v64, v64, v67, v55\n"
        "v_sub_f32 v66, v55, v64\n"
        "v_exp_f32 v66, v66\n"
        "v_mov_b32 v55, v64\n"
        "v_pk_mul_f32 v[88:89], v[88:89], v[66:67] op_sel_hi:[1,0]\n"
        "v_pk_mul_f32 v[90:91], v[90:91], v[66:67] op_sel_hi:[1,0]\n"
        "v_pk_mul_f32 v[92:93], v[92:93], v[66:67] op_sel_hi:[1,0]\n"
        "v_pk_mul_f32 v[94:95], v[94:95], v[66:67] op_sel_hi:[1,0]\n"
        "v_pk_mul_f32 v[96:97], v[96:97], v[66:67] op_sel_hi:[1,0]\n"
        "v_pk_mul_f32 v[98:99], v[98:99], v[66:67] op_sel_hi:[1,0]\n"
        "v_pk_mul_f32 v[100:101], v[100:101], v[66:67] op_sel_hi:[1,0]\n"
        "v_pk_mul_f32 v[102:103], v[102:103], v[66:67] op_sel_hi:[1,0]\n"
        "v_pk_mul_f32 v[104:105], v[104:105], v[66:67] op_sel_hi:[1,0]\n"
        "v_pk_mul_f32 v[106:107], v[106:107], v[66:67] op_sel_hi:[1,0]\n"
        "v_pk_mul_f32 v[108:109], v[108:109], v[66:67] op_sel_hi:[1,0]\n"
        "v_pk_mul_f32 v[110:111], v[110:111], v[66:67] op_sel_hi:[1,0]\n"
        "v_pk_mul_f32 v[112:113], v[112:113], v[66:67] op_sel_hi:[1,0]\n"
        "v_pk_mul_f32 v[114:115], v[114:115], v[66:67] op_sel_hi:[1,0]\n"
        "v_pk_mul_f32 v[116:117], v[116:117], v[66:67] op_sel_hi:[1,0]\n"
        "v_pk_mul_f32 v[118:119], v[118:119], v[66:67] op_sel_hi:[1,0]\n"
        "v_pk_mul_f32 v[120:121], v[120:121], v[66:67] op_sel_hi:[1,0]\n"
        "v_pk_mul_f32 v[122:123], v[122:123], v[66:67] op_sel_hi:[1,0]\n"
        "s_barrier\n"
        "ds_read_b128 v[72:75], v2 offset:4096\n"
        "ds_read_b128 v[76:79], v2 offset:5120\n"
        "s_branch X_PV\n"
        "APATH:\n"
        "s_lshl_b32 s42, s36, 18\n"
        "s_lshl_b32 s47, s29, 14\n"
        "s_add_u32 s42, s42, s47\n"
        "s_add_u32 s48, s8, s42\n"
        "s_addc_u32 s49, s9, 0\n"
        "s_add_u32 s47, s47, 32768\n"
        "s_load_dwordx4 s[20:23], %0, 0x38\n"
        "s_load_dwordx2 s[24:25], %0, 0x48\n"
        "v_mov_b32 v17, v16\n"
        "v_mov_b32 v18, v16\n"
        "v_mov_b32 v19, v16\n"
        "v_lshrrev_b32 v44, 1, v3\n"
        "v_or_b32 v44, v44, v5\n"
        "v_lshlrev_b32 v45, 3, v6\n"
        "v_lshlrev_b32 v46, 1, v3\n"
        "v_and_b32 v46, 32, v46\n"
        "v_xor_b32 v45, v45, v46\n"
        "v_lshl_or_b32 v7, v44, 6, v45\n"
        "v_or_b32 v7, s46, v7\n"
        "v_lshlrev_b32 v44, 1, v4\n"
        "v_and_b32 v44, 14, v44\n"
        "v_lshrrev_b32 v45, 3, v4\n"
        "v_or_b32 v44, v44, v45\n"
        "v_lshlrev_b32 v45, 1, v3\n"
        "v_lshlrev_b32 v46, 4, v44\n"
        "v_xor_b32 v45, v45, v46\n"
        "v_lshl_or_b32 v45, v44, 10, v45\n"
        "v_add_u32 v48, s47, v45\n"
        "v_xor_b32 v49, 16, v48\n"
        "v_xor_b32 v50, 128, v48\n"
        "v_xor_b32 v51, 144, v48\n"
        "s_lshr_b32 s42, s29, 1\n"
        "s_lshl_b32 s42, s42, 10\n"
        "s_and_b32 s43, s29, 1\n"
        "s_lshl_b32 s43, s43, 3\n"
        "s_add_u32 s42, s42, s43\n"
        "v_add_u32 v61, s42, v2\n"
        "s_lshl_b32 s42, s29, 2\n"
        "v_lshl_add_u32 v62, v4, 4, s42\n"
        "s_sleep 6\n"
        "s_add_u32 m0, s47, 0\n"
        "s_nop 0\n"
        "global_load_lds_dwordx4 v2, s[48:49] offset:0\n"
        "v_xor_b32 v60, 0x10, v2\n"
        "global_load_lds_dwordx4 v60, s[48:49] offset:1024\n"
        "v_xor_b32 v60, 0x20, v2\n"
        "global_load_lds_dwordx4 v60, s[48:49] offset:2048\n"
        "v_xor_b32 v60, 0x30, v2\n"
        "global_load_lds_dwordx4 v60, s[48:49] offset:3072\n"
        "s_add_u32 m0, s47, 4096\n"
        "v_xor_b32 v60, 0x1040, v2\n"
        "global_load_lds_dwordx4 v60, s[48:49] offset:0\n"
        "v_xor_b32 v60, 0x1050, v2\n"
        "global_load_lds_dwordx4 v60, s[48:49] offset:1024\n"
        "v_xor_b32 v60, 0x1060, v2\n"
        "global_load_lds_dwordx4 v60, s[48:49] offset:2048\n"
        "v_xor_b32 v60, 0x1070, v2\n"
        "global_load_lds_dwordx4 v60, s[48:49] offset:3072\n"
        "s_lshl_b32 s42, s29, 8\n"
        "s_add_u32 s42, s42, 2048\n"
        "v_lshlrev_b32 v46, 4, v4\n"
        "v_lshl_or_b32 v46, v3, 6, v46\n"
        "v_add_u32 v32, s42, v46\n"
        "v_and_b32 v46, 32, v1\n"
        "v_lshl_or_b32 v46, v4, 1, v46\n"
        "s_lshl_b32 s42, s29, 6\n"
        "v_or_b32 v33, s42, v46\n"
        "s_lshl_b32 s42, s35, 7\n"
        "s_lshl_b32 s43, s28, 4\n"
        "s_add_u32 s42, s42, s43\n"
        "v_lshrrev_b32 v46, 3, v1\n"
        "v_add_u32 v46, s42, v46\n"
        "v_mul_u32_u24 v46, 18688, v46\n"
        "s_lshl_b32 s42, s30, 8\n"
        "s_lshl_b32 s43, s34, 4\n"
        "s_add_u32 s42, s42, s43\n"
        "s_add_u32 s42, s42, 1003520\n"
        "v_add_u32 v35, s42, v46\n"
        "s_lshl_b32 s42, s30, 11\n"
        "s_lshl_b32 s43, s34, 6\n"
        "s_add_u32 s42, s42, s43\n"
        "s_add_u32 s42, s42, 987136\n"
        "v_and_b32 v47, 4, v1\n"
        "v_lshlrev_b32 v47, 8, v47\n"
        "v_lshl_or_b32 v47, v6, 4, v47\n"
        "v_add3_u32 v34, v46, v47, s42\n"
        "s_add_u32 s72, s4, 149504\n"
        "s_addc_u32 s73, s5, 0\n"
        "v_lshrrev_b32 v46, 5, v1\n"
        "s_lshl_b32 s42, s28, 1\n"
        "v_add_u32 v46, s42, v46\n"
        "v_lshlrev_b32 v37, 2, v46\n"
        "v_mul_u32_u24 v46, 144, v46\n"
        "v_and_b32 v47, 4, v1\n"
        "v_lshlrev_b32 v47, 4, v47\n"
        "v_lshl_or_b32 v47, v6, 3, v47\n"
        "v_and_b32 v39, 16, v1\n"
        "v_lshl_or_b32 v47, v39, 1, v47\n"
        "v_add_u32 v36, v46, v47\n"
        "v_cmp_eq_u32 vcc, 0, v39\n"
        "v_mov_b32 v46, 7360\n"
        "s_nop 0\n"
        "v_cndmask_b32 v37, v46, v37, vcc\n"
        "s_lshl_b32 s42, s28, 2\n"
        "v_mov_b32 v38, s42\n"
        "v_and_b32 v46, 1, v1\n"
        "v_lshlrev_b32 v46, 4, v46\n"
        "v_mov_b32 v47, 0xffff\n"
        "v_lshlrev_b32 v46, v46, v47\n"
        "v_bfe_u32 v47, v1, 1, 2\n"
        "v_cmp_eq_u32 vcc, 0, v47\n"
        "s_nop 1\n"
        "v_cndmask_b32 v40, 0, v46, vcc\n"
        "v_cmp_eq_u32 vcc, 1, v47\n"
        "s_nop 1\n"
        "v_cndmask_b32 v41, 0, v46, vcc\n"
        "v_cmp_eq_u32 vcc, 2, v47\n"
        "s_nop 1\n"
        "v_cndmask_b32 v42, 0, v46, vcc\n"
        "v_cmp_eq_u32 vcc, 3, v47\n"
        "s_nop 1\n"
        "v_cndmask_b32 v43, 0, v46, vcc\n"
        "s_lshr_b32 s37, s74, 6\n"
        "s_lshl_b32 s42, s30, 2\n"
        "s_add_u32 s37, s37, s42\n"
        "s_bfe_u32 s40, s74, 0x30003\n"
        "v_cmp_eq_u32 vcc, 0, v4\n"
        "s_waitcnt vmcnt(8) lgkmcnt(0)\n"
        "v_dot2_f32_bf16 v14, v20, v16, 0\n"
        "v_dot2_f32_bf16 v14, v21, v16, v14\n"
        "v_dot2_f32_bf16 v14, v22, v16, v14\n"
        "v_dot2_f32_bf16 v14, v23, v16, v14\n"
        "v_readfirstlane_b32 s38, v13\n"
        "v_cndmask_b32 v24, v16, v24, vcc\n"
        "v_cndmask_b32 v25, v16, v25, vcc\n"
        "v_add_f32_dpp v14, v14, v14 row_ror:8 row_mask:0xf bank_mask:0xf\n"
        "v_cndmask_b32 v26, v16, v26, vcc\n"
        "v_cndmask_b32 v27, v16, v27, vcc\n"
        "v_add_f32_dpp v14, v14, v14 row_ror:4 row_mask:0xf bank_mask:0xf\n"
        "v_cndmask_b32 v28, v16, v28, vcc\n"
        "v_cndmask_b32 v29, v16, v29, vcc\n"
        "v_add_f32_dpp v14, v14, v14 row_ror:2 row_mask:0xf bank_mask:0xf\n"
        "v_cndmask_b32 v30, v16, v30, vcc\n"
        "v_cndmask_b32 v31, v16, v31, vcc\n"
        "v_add_f32_dpp v14, v14, v14 row_ror:1 row_mask:0xf bank_mask:0xf\n"
        "v_mov_b32 v47, v14\n"
        "s_add_u32 s38, s38, 1\n"
        "s_not_b32 s39, s38\n"
        "v_permlane16_swap_b32 v14, v47\n"
        "v_add_f32 v14, v14, v47\n"
        "v_mov_b32 v47, v14\n"
        "s_nop 1\n"
        "v_permlane32_swap_b32 v14, v47\n"
        "v_add_f32 v14, v14, v47\n"
        "v_mul_f32 v14, 0x3b000000, v14\n"
        "v_lshlrev_b32 v128, 16, v20\n"
        "v_and_b32 v129, 0xffff0000, v20\n"
        "v_lshlrev_b32 v130, 16, v21\n"
        "v_and_b32 v131, 0xffff0000, v21\n"
        "v_lshlrev_b32 v132, 16, v22\n"
        "v_and_b32 v133, 0xffff0000, v22\n"
        "v_lshlrev_b32 v134, 16, v23\n"
        "v_and_b32 v135, 0xffff0000, v23\n"
        "v_sub_f32 v128, v128, v14\n"
        "v_sub_f32 v129, v129, v14\n"
        "v_sub_f32 v130, v130, v14\n"
        "v_sub_f32 v131, v131, v14\n"
        "v_sub_f32 v132, v132, v14\n"
        "v_sub_f32 v133, v133, v14\n"
        "v_sub_f32 v134, v134, v14\n"
        "v_sub_f32 v135, v135, v14\n"
        "v_mul_f32 v15, v128, v128\n"
        "v_fmac_f32 v15, v129, v129\n"
        "v_fmac_f32 v15, v130, v130\n"
        "v_fmac_f32 v15, v131, v131\n"
        "v_fmac_f32 v15, v132, v132\n"
        "v_fmac_f32 v15, v133, v133\n"
        "v_fmac_f32 v15, v134, v134\n"
        "v_fmac_f32 v15, v135, v135\n"
        "s_nop 1\n"
        "v_add_f32_dpp v15, v15, v15 row_ror:8 row_mask:0xf bank_mask:0xf\n"
        "s_nop 1\n"
        "v_add_f32_dpp v15, v15, v15 row_ror:4 row_mask:0xf bank_mask:0xf\n"
        "s_nop 1\n"
        "v_add_f32_dpp v15, v15, v15 row_ror:2 row_mask:0xf bank_mask:0xf\n"
        "s_nop 1\n"
        "v_add_f32_dpp v15, v15, v15 row_ror:1 row_mask:0xf bank_mask:0xf\n"
        "v_mov_b32 v128, v15\n"
        "s_nop 1\n"
        "v_permlane16_swap_b32 v15, v128\n"
        "v_add_f32 v15, v15, v128\n"
        "v_mov_b32 v128, v15\n"
        "s_nop 1\n"
        "v_permlane32_swap_b32 v15, v128\n"
        "v_add_f32 v15, v15, v128\n"
        "v_mov_b32 v128, 0x3727c5ac\n"
        "v_fmac_f32 v128, 0x3b000000, v15\n"
        "v_rsq_f32 v15, v128\n"
        "s_mul_i32 s75, s31, 43\n"
        "s_add_u32 s76, s75, 43\n"
        "s_lshr_b32 s75, s75, 3\n"
        "s_lshr_b32 s76, s76, 3\n"
        "s_sub_u32 s76, s76, s75\n"
        "s_mul_i32 s42, s30, 176128\n"
        "s_lshl_b32 s43, s75, 4\n"
        "s_add_u32 s42, s42, s43\n"
        "s_add_u32 s82, s14, s42\n"
        "s_addc_u32 s83, s15, 0\n"
        "s_add_u32 s84, s12, s70\n"
        "s_addc_u32 s85, s13, 0\n"
        "v_and_b32 v46, 1, v1\n"
        "s_lshl_b32 s42, s29, 1\n"
        "v_or_b32 v46, s42, v46\n"
        "v_cmp_gt_u32 vcc, s76, v46\n"
        "v_lshrrev_b32 v47, 1, v1\n"
        "v_mul_u32_u24 v47, 2752, v47\n"
        "v_cndmask_b32 v46, 0, v46, vcc\n"
        "v_lshl_add_u32 v20, v46, 4, v47\n"
        "v_add_u32 v21, 88064, v20\n"
        "s_mul_i32 s75, s37, 43\n"
        "s_add_u32 s76, s75, 43\n"
        "s_lshr_b32 s75, s75, 3\n"
        "s_lshr_b32 s76, s76, 3\n"
        "s_sub_u32 s76, s76, s75\n"
        "s_lshl_b32 s43, s40, 7\n"
        "s_lshl_b32 s42, s29, 1\n"
        "s_add_u32 s42, s42, 0\n"
        "s_cmp_lt_u32 s42, s76\n"
        "s_cselect_b32 s42, s42, 0\n"
        "s_add_u32 s42, s42, s75\n"
        "s_lshl_b32 s42, s42, 13\n"
        "s_add_u32 s42, s42, s43\n"
        "s_add_u32 s86, s22, s42\n"
        "s_addc_u32 s87, s23, 0\n"
        "s_lshl_b32 s42, s29, 1\n"
        "s_add_u32 s42, s42, 1\n"
        "s_cmp_lt_u32 s42, s76\n"
        "s_cselect_b32 s42, s42, 0\n"
        "s_add_u32 s42, s42, s75\n"
        "s_lshl_b32 s42, s42, 13\n"
        "s_add_u32 s42, s42, s43\n"
        "s_add_u32 s88, s22, s42\n"
        "s_addc_u32 s89, s23, 0\n"
        "v_lshrrev_b32 v46, 3, v1\n"
        "v_and_b32 v47, 7, v1\n"
        "v_bitop3_b32 v47, v46, v47, 2 bitop3:0x6c\n"
        "v_lshlrev_b32 v47, 4, v47\n"
        "v_lshl_or_b32 v23, v46, 10, v47\n"
        "s_lshl_b32 s42, s29, 2\n"
        "v_mov_b32 v22, s42\n"
        "s_mov_b32 s68, 2\n"
        "v_mov_b32 v44, 8\n"
        "v_mov_b32 v45, 1\n"
        "v_lshlrev_b32 v46, 4, v4\n"
        "s_mov_b32 s45, 0x41200000\n"
        "s_add_u32 m0, s47, 8192\n"
        "v_xor_b32 v60, 0x2080, v2\n"
        "s_waitcnt vmcnt(7)\n"
        "global_load_lds_dwordx4 v60, s[48:49] offset:0\n"
        "v_xor_b32 v60, 0x2090, v2\n"
        "s_waitcnt vmcnt(7)\n"
        "global_load_lds_dwordx4 v60, s[48:49] offset:1024\n"
        "v_xor_b32 v60, 0x20a0, v2\n"
        "s_waitcnt vmcnt(7)\n"
        "global_load_lds_dwordx4 v60, s[48:49] offset:2048\n"
        "v_xor_b32 v60, 0x20b0, v2\n"
        "s_waitcnt vmcnt(7)\n"
        "global_load_lds_dwordx4 v60, s[48:49] offset:3072\n"
        "s_add_u32 m0, s47, 12288\n"
        "v_xor_b32 v60, 0x30c0, v2\n"
        "s_waitcnt vmcnt(7)\n"
        "global_load_lds_dwordx4 v60, s[48:49] offset:0\n"
        "v_xor_b32 v60, 0x30d0, v2\n"
        "s_waitcnt vmcnt(7)\n"
        "global_load_lds_dwordx4 v60, s[48:49] offset:1024\n"
        "v_xor_b32 v60, 0x30e0, v2\n"
        "s_waitcnt vmcnt(7)\n"
        "global_load_lds_dwordx4 v60, s[48:49] offset:2048\n"
        "v_xor_b32 v60, 0x30f0, v2\n"
        "s_waitcnt vmcnt(7)\n"
        "global_load_lds_dwordx4 v60, s[48:49] offset:3072\n"
        "s_add_u32 m0, s47, 65536\n"
        "v_xor_b32 v60, 0x10000, v2\n"
        "s_waitcnt vmcnt(7)\n"
        "global_load_lds_dwordx4 v60, s[48:49] offset:0\n"
        "v_xor_b32 v60, 0x10010, v2\n"
        "s_waitcnt vmcnt(7)\n"
        "global_load_lds_dwordx4 v60, s[48:49] offset:1024\n"
        "v_xor_b32 v60, 0x10020, v2\n"
        "s_waitcnt vmcnt(7)\n"
        "global_load_lds_dwordx4 v60, s[48:49] offset:2048\n"
        "v_xor_b32 v60, 0x10030, v2\n"
        "s_waitcnt vmcnt(7)\n"
        "global_load_lds_dwordx4 v60, s[48:49] offset:3072\n"
        "s_add_u32 m0, s47, 69632\n"
        "v_xor_b32 v60, 0x11040, v2\n"
        "s_waitcnt vmcnt(7)\n"
        "global_load_lds_dwordx4 v60, s[48:49] offset:0\n"
        "v_xor_b32 v60, 0x11050, v2\n"
        "s_waitcnt vmcnt(7)\n"
        "global_load_lds_dwordx4 v60, s[48:49] offset:1024\n"
        "v_xor_b32 v60, 0x11060, v2\n"
        "s_waitcnt vmcnt(7)\n"
        "global_load_lds_dwordx4 v60, s[48:49] offset:2048\n"
        "v_xor_b32 v60, 0x11070, v2\n"
        "s_waitcnt vmcnt(7)\n"
        "global_load_lds_dwordx4 v60, s[48:49] offset:3072\n"
        "ds_write_b32 v22, v45 offset:32\n"
        "ds_read_b128 v[128:131], v48 offset:0\n"
        "ds_read_b128 v[132:135], v49 offset:0\n"
        "ds_read_b128 v[136:139], v50 offset:0\n"
        "ds_read_b128 v[140:143], v51 offset:0\n"
        "ds_read_b128 v[144:147], v48 offset:256\n"
        "ds_read_b128 v[148:151], v49 offset:256\n"
        "ds_read_b128 v[152:155], v50 offset:256\n"
        "ds_read_b128 v[156:159], v51 offset:256\n"
        "ds_read_b128 v[160:163], v48 offset:512\n"
        "ds_read_b128 v[164:167], v49 offset:512\n"
        "ds_read_b128 v[168:171], v50 offset:512\n"
        "ds_read_b128 v[172:175], v51 offset:512\n"
        "ds_read_b128 v[176:179], v48 offset:768\n"
        "ds_read_b128 v[180:183], v49 offset:768\n"
        "ds_read_b128 v[184:187], v50 offset:768\n"
        "ds_read_b128 v[188:191], v51 offset:768\n"
        "v_add_u32 v48, 0x10000, v48\n"
        "v_add_u32 v49, 0x10000, v49\n"
        "v_add_u32 v50, 0x10000, v50\n"
        "v_add_u32 v51, 0x10000, v51\n"
        "s_add_u32 m0, s47, 73728\n"
        "v_xor_b32 v60, 0x12080, v2\n"
        "s_waitcnt vmcnt(7)\n"
        "global_load_lds_dwordx4 v60, s[48:49] offset:0\n"
        "v_xor_b32 v60, 0x12090, v2\n"
        "s_waitcnt vmcnt(7)\n"
        "global_load_lds_dwordx4 v60, s[48:49] offset:1024\n"
        "v_xor_b32 v60, 0x120a0, v2\n"
        "s_waitcnt vmcnt(7)\n"
        "global_load_lds_dwordx4 v60, s[48:49] offset:2048\n"
        "v_xor_b32 v60, 0x120b0, v2\n"
        "s_waitcnt vmcnt(7)\n"
        "global_load_lds_dwordx4 v60, s[48:49] offset:3072\n"
        "s_add_u32 m0, s47, 77824\n"
        "v_xor_b32 v60, 0x130c0, v2\n"
        "s_waitcnt vmcnt(7)\n"
        "global_load_lds_dwordx4 v60, s[48:49] offset:0\n"
        "v_xor_b32 v60, 0x130d0, v2\n"
        "s_waitcnt vmcnt(7)\n"
        "global_load_lds_dwordx4 v60, s[48:49] offset:1024\n"
        "v_xor_b32 v60, 0x130e0, v2\n"
        "s_waitcnt vmcnt(7)\n"
        "global_load_lds_dwordx4 v60, s[48:49] offset:2048\n"
        "v_xor_b32 v60, 0x130f0, v2\n"
        "s_waitcnt vmcnt(7)\n"
        "global_load_lds_dwordx4 v60, s[48:49] offset:3072\n"
        "s_mov_b32 s41, 0\n"
        "FREE0:\n"
        "ds_read_b128 v[52:55], v12 offset:64\n"
        "s_add_u32 s41, s41, 1\n"
        "s_cmp_gt_u32 s41, 524288\n"
        "s_waitcnt lgkmcnt(0)\n"
        "v_add3_u32 v52, v52, v53, v54\n"
        "v_add_u32 v52, v52, v55\n"
        "v_cmp_ne_u32 vcc, 4, v52\n"
        "s_cbranch_scc1 FREE0_D\n"
        "s_cbranch_vccz FREE0_D\n"
        "s_sleep 2\n"
        "s_branch FREE0\n"
        "FREE0_D:\n"
        "s_add_u32 m0, s47, 0\n"
        "v_xor_b32 v60, 0x20000, v2\n"
        "s_waitcnt vmcnt(7)\n"
        "global_load_lds_dwordx4 v60, s[48:49] offset:0\n"
        "v_xor_b32 v60, 0x20010, v2\n"
        "s_waitcnt vmcnt(7)\n"
        "global_load_lds_dwordx4 v60, s[48:49] offset:1024\n"
        "v_xor_b32 v60, 0x20020, v2\n"
        "s_waitcnt vmcnt(7)\n"
        "global_load_lds_dwordx4 v60, s[48:49] offset:2048\n"
        "v_xor_b32 v60, 0x20030, v2\n"
        "s_waitcnt vmcnt(7)\n"
        "global_load_lds_dwordx4 v60, s[48:49] offset:3072\n"
        "s_add_u32 m0, s47, 4096\n"
        "v_xor_b32 v60, 0x21040, v2\n"
        "s_waitcnt vmcnt(7)\n"
        "global_load_lds_dwordx4 v60, s[48:49] offset:0\n"
        "v_xor_b32 v60, 0x21050, v2\n"
        "s_waitcnt vmcnt(7)\n"
        "global_load_lds_dwordx4 v60, s[48:49] offset:1024\n"
        "v_xor_b32 v60, 0x21060, v2\n"
        "s_waitcnt vmcnt(7)\n"
        "global_load_lds_dwordx4 v60, s[48:49] offset:2048\n"
        "v_xor_b32 v60, 0x21070, v2\n"
        "s_waitcnt vmcnt(7)\n"
        "global_load_lds_dwordx4 v60, s[48:49] offset:3072\n"
        "ds_write_b32 v22, v45 offset:48\n"
        "ds_read_b128 v[192:195], v48 offset:0\n"
        "ds_read_b128 v[196:199], v49 offset:0\n"
        "ds_read_b128 v[200:203], v50 offset:0\n"
        "ds_read_b128 v[204:207], v51 offset:0\n"
        "ds_read_b128 v[208:211], v48 offset:256\n"
        "ds_read_b128 v[212:215], v49 offset:256\n"
        "ds_read_b128 v[216:219], v50 offset:256\n"
        "ds_read_b128 v[220:223], v51 offset:256\n"
        "ds_read_b128 v[224:227], v48 offset:512\n"
        "ds_read_b128 v[228:231], v49 offset:512\n"
        "ds_read_b128 v[232:235], v50 offset:512\n"
        "ds_read_b128 v[236:239], v51 offset:512\n"
        "ds_read_b128 v[240:243], v48 offset:768\n"
        "ds_read_b128 v[244:247], v49 offset:768\n"
        "ds_read_b128 v[248:251], v50 offset:768\n"
        "ds_read_b128 v[252:255], v51 offset:768\n"
        "v_add_u32 v48, 0xffff0000, v48\n"
        "v_add_u32 v49, 0xffff0000, v49\n"
        "v_add_u32 v50, 0xffff0000, v50\n"
        "v_add_u32 v51, 0xffff0000, v51\n"
        "s_add_u32 m0, s47, 8192\n"
        "v_xor_b32 v60, 0x22080, v2\n"
        "s_waitcnt vmcnt(7)\n"
        "global_load_lds_dwordx4 v60, s[48:49] offset:0\n"
        "v_xor_b32 v60, 0x22090, v2\n"
        "s_waitcnt vmcnt(7)\n"
        "global_load_lds_dwordx4 v60, s[48:49] offset:1024\n"
        "v_xor_b32 v60, 0x220a0, v2\n"
        "s_waitcnt vmcnt(7)\n"
        "global_load_lds_dwordx4 v60, s[48:49] offset:2048\n"
        "v_xor_b32 v60, 0x220b0, v2\n"
        "s_waitcnt vmcnt(7)\n"
        "global_load_lds_dwordx4 v60, s[48:49] offset:3072\n"
        "s_add_u32 m0, s47, 12288\n"
        "v_xor_b32 v60, 0x230c0, v2\n"
        "s_waitcnt vmcnt(7)\n"
        "global_load_lds_dwordx4 v60, s[48:49] offset:0\n"
        "v_xor_b32 v60, 0x230d0, v2\n"
        "s_waitcnt vmcnt(7)\n"
        "global_load_lds_dwordx4 v60, s[48:49] offset:1024\n"
        "v_xor_b32 v60, 0x230e0, v2\n"
        "s_waitcnt vmcnt(7)\n"
        "global_load_lds_dwordx4 v60, s[48:49] offset:2048\n"
        "v_xor_b32 v60, 0x230f0, v2\n"
        "s_waitcnt vmcnt(7)\n"
        "global_load_lds_dwordx4 v60, s[48:49] offset:3072\n"
        "s_mov_b32 s41, 0\n"
        "FREE1:\n"
        "ds_read_b128 v[52:55], v12 offset:80\n"
        "s_add_u32 s41, s41, 1\n"
        "s_cmp_gt_u32 s41, 524288\n"
        "s_waitcnt lgkmcnt(0)\n"
        "v_add3_u32 v52, v52, v53, v54\n"
        "v_add_u32 v52, v52, v55\n"
        "v_cmp_ne_u32 vcc, 4, v52\n"
        "s_cbranch_scc1 FREE1_D\n"
        "s_cbranch_vccz FREE1_D\n"
        "s_sleep 2\n"
        "s_branch FREE1\n"
        "FREE1_D:\n"
        "s_add_u32 m0, s47, 65536\n"
        "v_xor_b32 v60, 0x30000, v2\n"
        "s_waitcnt vmcnt(7)\n"
        "global_load_lds_dwordx4 v60, s[48:49] offset:0\n"
        "v_xor_b32 v60, 0x30010, v2\n"
        "s_waitcnt vmcnt(7)\n"
        "global_load_lds_dwordx4 v60, s[48:49] offset:1024\n"
        "v_xor_b32 v60, 0x30020, v2\n"
        "s_waitcnt vmcnt(7)\n"
        "global_load_lds_dwordx4 v60, s[48:49] offset:2048\n"
        "v_xor_b32 v60, 0x30030, v2\n"
        "s_waitcnt vmcnt(7)\n"
        "global_load_lds_dwordx4 v60, s[48:49] offset:3072\n"
        "s_setprio 1\n"
        "s_barrier\n"
        "ds_read_b128 v[64:67], v2 offset:16384\n"
        "ds_read_b128 v[68:71], v2 offset:17408\n"
        "ds_read_b128 v[72:75], v2 offset:18432\n"
        "ds_read_b128 v[76:79], v2 offset:19456\n"
        "ds_read_b128 v[80:83], v2 offset:20480\n"
        "ds_read_b128 v[84:87], v2 offset:21504\n"
        "ds_read_b128 v[88:91], v2 offset:22528\n"
        "ds_read_b128 v[92:95], v2 offset:23552\n"
        "ds_read_b128 v[96:99], v2 offset:24576\n"
        "ds_read_b128 v[100:103], v2 offset:25600\n"
        "ds_read_b128 v[104:107], v2 offset:26624\n"
        "ds_read_b128 v[108:111], v2 offset:27648\n"
        "ds_read_b128 v[112:115], v2 offset:28672\n"
        "ds_read_b128 v[116:119], v2 offset:29696\n"
        "ds_read_b128 v[120:123], v2 offset:30720\n"
        "ds_read_b128 v[124:127], v2 offset:31744\n"
        "s_waitcnt lgkmcnt(14)\n"
        "v_mfma_f32_16x16x32_bf16 v[52:55], v[128:131], v[64:67], 0\n"
        "v_mfma_f32_16x16x32_bf16 v[52:55], v[132:135], v[68:71], v[52:55]\n"
        "s_waitcnt lgkmcnt(12)\n"
        "v_mfma_f32_16x16x32_bf16 v[52:55], v[136:139], v[72:75], v[52:55]\n"
        "v_mfma_f32_16x16x32_bf16 v[52:55], v[140:143], v[76:79], v[52:55]\n"
        "s_waitcnt lgkmcnt(8)\n"
        "v_mfma_f32_16x16x32_bf16 v[52:55], v[144:147], v[80:83], v[52:55]\n"
        "v_mfma_f32_16x16x32_bf16 v[52:55], v[148:151], v[84:87], v[52:55]\n"
        "v_mfma_f32_16x16x32_bf16 v[52:55], v[152:155], v[88:91], v[52:55]\n"
        "v_mfma_f32_16x16x32_bf16 v[52:55], v[156:159], v[92:95], v[52:55]\n"
        "s_waitcnt lgkmcnt(4)\n"
        "v_mfma_f32_16x16x32_bf16 v[52:55], v[160:163], v[96:99], v[52:55]\n"
        "v_mfma_f32_16x16x32_bf16 v[52:55], v[164:167], v[100:103], v[52:55]\n"
        "v_mfma_f32_16x16x32_bf16 v[52:55], v[168:171], v[104:107], v[52:55]\n"
        "v_mfma_f32_16x16x32_bf16 v[52:55], v[172:175], v[108:111], v[52:55]\n"
        "s_waitcnt lgkmcnt(0)\n"
        "v_mfma_f32_16x16x32_bf16 v[52:55], v[176:179], v[112:115], v[52:55]\n"
        "v_mfma_f32_16x16x32_bf16 v[52:55], v[180:183], v[116:119], v[52:55]\n"
        "v_mfma_f32_16x16x32_bf16 v[52:55], v[184:187], v[120:123], v[52:55]\n"
        "v_mfma_f32_16x16x32_bf16 v[52:55], v[188:191], v[124:127], v[52:55]\n"
        "s_waitcnt vmcnt(4)\n"
        "v_mfma_f32_16x16x32_bf16 v[56:59], v[192:195], v[64:67], 0\n"
        "ds_read_b128 v[128:131], v48 offset:0\n"
        "v_mfma_f32_16x16x32_bf16 v[56:59], v[196:199], v[68:71], v[56:59]\n"
        "ds_read_b128 v[132:135], v49 offset:0\n"
        "v_mfma_f32_16x16x32_bf16 v[56:59], v[200:203], v[72:75], v[56:59]\n"
        "ds_read_b128 v[136:139], v50 offset:0\n"
        "v_mfma_f32_16x16x32_bf16 v[56:59], v[204:207], v[76:79], v[56:59]\n"
        "ds_read_b128 v[140:143], v51 offset:0\n"
        "v_mfma_f32_16x16x32_bf16 v[56:59], v[208:211], v[80:83], v[56:59]\n"
        "ds_read_b128 v[144:147], v48 offset:256\n"
        "v_mfma_f32_16x16x32_bf16 v[56:59], v[212:215], v[84:87], v[56:59]\n"
        "ds_read_b128 v[148:151], v49 offset:256\n"
        "v_mfma_f32_16x16x32_bf16 v[56:59], v[216:219], v[88:91], v[56:59]\n"
        "ds_read_b128 v[152:155], v50 offset:256\n"
        "v_mfma_f32_16x16x32_bf16 v[56:59], v[220:223], v[92:95], v[56:59]\n"
        "ds_read_b128 v[156:159], v51 offset:256\n"
        "v_mfma_f32_16x16x32_bf16 v[56:59], v[224:227], v[96:99], v[56:59]\n"
        "ds_read_b128 v[160:163], v48 offset:512\n"
        "v_mfma_f32_16x16x32_bf16 v[56:59], v[228:231], v[100:103], v[56:59]\n"
        "ds_read_b128 v[164:167], v49 offset:512\n"
        "v_mfma_f32_16x16x32_bf16 v[56:59], v[232:235], v[104:107], v[56:59]\n"
        "ds_read_b128 v[168:171], v50 offset:512\n"
        "v_mfma_f32_16x16x32_bf16 v[56:59], v[236:239], v[108:111], v[56:59]\n"
        "ds_read_b128 v[172:175], v51 offset:512\n"
        "v_mfma_f32_16x16x32_bf16 v[56:59], v[240:243], v[112:115], v[56:59]\n"
        "ds_read_b128 v[176:179], v48 offset:768\n"
        "v_mfma_f32_16x16x32_bf16 v[56:59], v[244:247], v[116:119], v[56:59]\n"
        "ds_read_b128 v[180:183], v49 offset:768\n"
        "v_mfma_f32_16x16x32_bf16 v[56:59], v[248:251], v[120:123], v[56:59]\n"
        "ds_read_b128 v[184:187], v50 offset:768\n"
        "v_mfma_f32_16x16x32_bf16 v[56:59], v[252:255], v[124:127], v[56:59]\n"
        "ds_read_b128 v[188:191], v51 offset:768\n"
        "s_waitcnt lgkmcnt(8)\n"
        "v_mfma_f32_16x16x32_bf16 v[192:195], v[128:131], v[64:67], 0\n"
        "v_mfma_f32_16x16x32_bf16 v[192:195], v[132:135], v[68:71], v[192:195]\n"
        "v_mfma_f32_16x16x32_bf16 v[192:195], v[136:139], v[72:75], v[192:195]\n"
        "v_mfma_f32_16x16x32_bf16 v[192:195], v[140:143], v[76:79], v[192:195]\n"
        "v_mfma_f32_16x16x32_bf16 v[192:195], v[144:147], v[80:83], v[192:195]\n"
        "v_mfma_f32_16x16x32_bf16 v[192:195], v[148:151], v[84:87], v[192:195]\n"
        "v_mfma_f32_16x16x32_bf16 v[192:195], v[152:155], v[88:91], v[192:195]\n"
        "v_mfma_f32_16x16x32_bf16 v[192:195], v[156:159], v[92:95], v[192:195]\n"
        "s_waitcnt lgkmcnt(4)\n"
        "v_mfma_f32_16x16x32_bf16 v[192:195], v[160:163], v[96:99], v[192:195]\n"
        "v_max3_f32 v47, v52, v53, v54\n"
        "v_mfma_f32_16x16x32_bf16 v[192:195], v[164:167], v[100:103], v[192:195]\n"
        "v_max3_f32 v47, v47, v55, v56\n"
        "v_mfma_f32_16x16x32_bf16 v[192:195], v[168:171], v[104:107], v[192:195]\n"
        "v_max3_f32 v47, v47, v57, v58\n"
        "v_mfma_f32_16x16x32_bf16 v[192:195], v[172:175], v[108:111], v[192:195]\n"
        "s_waitcnt lgkmcnt(0)\n"
        "v_mfma_f32_16x16x32_bf16 v[192:195], v[176:179], v[112:115], v[192:195]\n"
        "v_mfma_f32_16x16x32_bf16 v[192:195], v[180:183], v[116:119], v[192:195]\n"
        "v_mfma_f32_16x16x32_bf16 v[192:195], v[184:187], v[120:123], v[192:195]\n"
        "v_mfma_f32_16x16x32_bf16 v[192:195], v[188:191], v[124:127], v[192:195]\n"
        "v_add_u32 v48, 0x10000, v48\n"
        "v_add_u32 v49, 0x10000, v49\n"
        "v_add_u32 v50, 0x10000, v50\n"
        "v_add_u32 v51, 0x10000, v51\n"
        "s_lshl_b32 s42, s29, 11\n"
        "s_add_u32 m0, s42, 8192\n"
        "s_nop 1\n"
        "v_max3_f32 v47, v47, v59, v192\n"
        "v_max3_f32 v47, v47, v193, v194\n"
        "v_max_f32 v47, v47, v195\n"
        "v_mov_b32 v128, v47\n"
        "s_nop 1\n"
        "v_permlane16_swap_b32 v47, v128\n"
        "v_max_f32 v47, v47, v128\n"
        "v_mov_b32 v128, v47\n"
        "s_nop 1\n"
        "v_permlane32_swap_b32 v47, v128\n"
        "v_max_f32 v47, v47, v128\n"
        "ds_write_b32 v62, v47 offset:256\n"
        "s_waitcnt lgkmcnt(0)\n"
        "s_barrier\n"
        "ds_read_b128 v[128:131], v46 offset:256\n"
        "s_waitcnt lgkmcnt(0)\n"
        "v_max3_f32 v47, v128, v129, v130\n"
        "v_max_f32 v39, v47, v131\n"
        "v_sub_f32 v52, v52, v39\n"
        "v_sub_f32 v53, v53, v39\n"
        "v_sub_f32 v54, v54, v39\n"
        "v_sub_f32 v55, v55, v39\n"
        "v_exp_f32 v52, v52\n"
        "v_exp_f32 v53, v53\n"
        "v_exp_f32 v54, v54\n"
        "v_exp_f32 v55, v55\n"
        "v_cvt_pk_bf16_f32 v52, v52, v53\n"
        "v_cvt_pk_bf16_f32 v53, v54, v55\n"
        "ds_write_b64 v61, v[52:53] offset:4096\n"
        "v_sub_f32 v56, v56, v39\n"
        "v_sub_f32 v57, v57, v39\n"
        "v_sub_f32 v58, v58, v39\n"
        "v_sub_f32 v59, v59, v39\n"
        "v_exp_f32 v56, v56\n"
        "v_exp_f32 v57, v57\n"
        "v_exp_f32 v58, v58\n"
        "v_exp_f32 v59, v59\n"
        "v_cvt_pk_bf16_f32 v56, v56, v57\n"
        "v_cvt_pk_bf16_f32 v57, v58, v59\n"
        "ds_write_b64 v61, v[56:57] offset:6144\n"
        "v_sub_f32 v192, v192, v39\n"
        "v_sub_f32 v193, v193, v39\n"
        "v_sub_f32 v194, v194, v39\n"
        "v_sub_f32 v195, v195, v39\n"
        "v_exp_f32 v192, v192\n"
        "v_exp_f32 v193, v193\n"
        "v_exp_f32 v194, v194\n"
        "v_exp_f32 v195, v195\n"
        "v_cvt_pk_bf16_f32 v192, v192, v193\n"
        "v_cvt_pk_bf16_f32 v193, v194, v195\n"
        "ds_write_b64 v61, v[192:193] offset:1536\n"
        "s_waitcnt lgkmcnt(0)\n"
        "s_barrier\n"
        "global_load_lds_dwordx4 v20, s[82:83]\n"
        "s_add_u32 m0, m0, 0x400\n"
        "s_nop 0\n"
        "global_load_lds_dwordx4 v21, s[82:83]\n"
        "s_add_u32 m0, s46, 16384\n"
        "s_nop 0\n"
        "global_load_lds_dwordx4 v8, s[84:85]\n"
        "s_add_u32 m0, m0, 0x400\n"
        "s_nop 0\n"
        "global_load_lds_dwordx4 v9, s[84:85]\n"
        "s_add_u32 m0, m0, 0x400\n"
        "s_nop 0\n"
        "global_load_lds_dwordx4 v10, s[84:85]\n"
        "s_add_u32 m0, m0, 0x400\n"
        "s_nop 0\n"
        "global_load_lds_dwordx4 v11, s[84:85]\n"
        "s_waitcnt vmcnt(6)\n"
        "s_barrier\n"
        "ds_read_b128 v[128:131], v48 offset:0\n"
        "ds_read_b128 v[132:135], v49 offset:0\n"
        "ds_read_b128 v[136:139], v50 offset:0\n"
        "ds_read_b128 v[140:143], v51 offset:0\n"
        "ds_read_b128 v[144:147], v48 offset:256\n"
        "ds_read_b128 v[148:151], v49 offset:256\n"
        "ds_read_b128 v[152:155], v50 offset:256\n"
        "ds_read_b128 v[156:159], v51 offset:256\n"
        "ds_read_b128 v[160:163], v48 offset:512\n"
        "ds_read_b128 v[164:167], v49 offset:512\n"
        "ds_read_b128 v[168:171], v50 offset:512\n"
        "ds_read_b128 v[172:175], v51 offset:512\n"
        "ds_read_b128 v[176:179], v48 offset:768\n"
        "ds_read_b128 v[180:183], v49 offset:768\n"
        "ds_read_b128 v[184:187], v50 offset:768\n"
        "ds_read_b128 v[188:191], v51 offset:768\n"
        "s_waitcnt lgkmcnt(14)\n"
        "v_mfma_f32_16x16x32_bf16 v[52:55], v[128:131], v[64:67], 0\n"
        "v_mfma_f32_16x16x32_bf16 v[52:55], v[132:135], v[68:71], v[52:55]\n"
        "s_waitcnt lgkmcnt(12)\n"
        "v_mfma_f32_16x16x32_bf16 v[52:55], v[136:139], v[72:75], v[52:55]\n"
        "v_mfma_f32_16x16x32_bf16 v[52:55], v[140:143], v[76:79], v[52:55]\n"
        "s_waitcnt lgkmcnt(8)\n"
        "v_mfma_f32_16x16x32_bf16 v[52:55], v[144:147], v[80:83], v[52:55]\n"
        "v_mfma_f32_16x16x32_bf16 v[52:55], v[148:151], v[84:87], v[52:55]\n"
        "v_mfma_f32_16x16x32_bf16 v[52:55], v[152:155], v[88:91], v[52:55]\n"
        "v_mfma_f32_16x16x32_bf16 v[52:55], v[156:159], v[92:95], v[52:55]\n"
        "s_waitcnt lgkmcnt(4)\n"
        "v_mfma_f32_16x16x32_bf16 v[52:55], v[160:163], v[96:99], v[52:55]\n"
        "v_mfma_f32_16x16x32_bf16 v[52:55], v[164:167], v[100:103], v[52:55]\n"
        "v_mfma_f32_16x16x32_bf16 v[52:55], v[168:171], v[104:107], v[52:55]\n"
        "v_mfma_f32_16x16x32_bf16 v[52:55], v[172:175], v[108:111], v[52:55]\n"
        "s_waitcnt lgkmcnt(0)\n"
        "v_mfma_f32_16x16x32_bf16 v[52:55], v[176:179], v[112:115], v[52:55]\n"
        "v_mfma_f32_16x16x32_bf16 v[52:55], v[180:183], v[116:119], v[52:55]\n"
        "v_mfma_f32_16x16x32_bf16 v[52:55], v[184:187], v[120:123], v[52:55]\n"
        "v_mfma_f32_16x16x32_bf16 v[52:55], v[188:191], v[124:127], v[52:55]\n"
        "s_nop 7\n"
        "v_sub_f32 v56, v52, v39\n"
        "v_sub_f32 v57, v53, v39\n"
        "v_sub_f32 v58, v54, v39\n"
        "v_sub_f32 v59, v55, v39\n"
        "v_max3_f32 v47, v56, v57, v58\n"
        "v_max_f32 v47, v47, v59\n"
        "v_exp_f32 v56, v56\n"
        "v_exp_f32 v57, v57\n"
        "v_exp_f32 v58, v58\n"
        "v_exp_f32 v59, v59\n"
        "v_cmp_nge_f32 vcc, s45, v47\n"
        "v_cvt_pk_bf16_f32 v56, v56, v57\n"
        "v_cvt_pk_bf16_f32 v57, v58, v59\n"
        "ds_write_b64 v61, v[56:57] offset:4096\n"
        "s_cbranch_vccz A_NOV\n"
        "ds_write_b32 v12, v45 offset:0\n"
        "A_NOV:\n"
        "s_waitcnt lgkmcnt(0)\n"
        "s_barrier\n"
        "ds_read_b32 v47, v12 offset:0\n"
        "s_waitcnt lgkmcnt(0)\n"
        "v_cmp_ne_u32 vcc, 0, v47\n"
        "s_cbranch_vccz A_NEXT\n"
        "v_max3_f32 v47, v52, v53, v54\n"
        "v_max_f32 v47, v47, v55\n"
        "v_mov_b32 v56, v47\n"
        "s_nop 1\n"
        "v_permlane16_swap_b32 v47, v56\n"
        "v_max_f32 v47, v47, v56\n"
        "v_mov_b32 v56, v47\n"
        "s_nop 1\n"
        "v_permlane32_swap_b32 v47, v56\n"
        "v_max_f32 v47, v47, v56\n"
        "ds_write_b32 v62, v47 offset:256\n"
        "s_waitcnt lgkmcnt(0)\n"
        "s_barrier\n"
        "ds_read_b128 v[56:59], v46 offset:256\n"
        "s_waitcnt lgkmcnt(0)\n"
        "v_max3_f32 v47, v56, v57, v58\n"
        "v_max3_f32 v39, v47, v59, v39\n"
        "v_sub_f32 v56, v52, v39\n"
        "v_sub_f32 v57, v53, v39\n"
        "v_sub_f32 v58, v54, v39\n"
        "v_sub_f32 v59, v55, v39\n"
        "v_exp_f32 v56, v56\n"
        "v_exp_f32 v57, v57\n"
        "v_exp_f32 v58, v58\n"
        "v_exp_f32 v59, v59\n"
        "v_cvt_pk_bf16_f32 v56, v56, v57\n"
        "v_cvt_pk_bf16_f32 v57, v58, v59\n"
        "ds_write_b64 v61, v[56:57] offset:4096\n"
        "s_waitcnt lgkmcnt(0)\n"
        "s_barrier\n"
        "A_NEXT:\n"
        "A_END:\n"
        "s_setprio 0\n"
        "v_lshrrev_b32 v44, 1, v3\n"
        "v_or_b32 v44, v44, v5\n"
        "v_lshlrev_b32 v45, 3, v6\n"
        "v_lshl_or_b32 v44, v44, 5, v45\n"
        "s_lshl_b32 s42, s29, 11\n"
        "v_add_u32 v44, s42, v44\n"
        "s_add_u32 m0, s42, 49152\n"
        "v_xor_b32 v22, 64, v23\n"
        "global_load_lds_dwordx4 v23, s[86:87]\n"
        "s_add_u32 m0, m0, 0x400\n"
        "s_nop 0\n"
        "global_load_lds_dwordx4 v22, s[88:89]\n"
        "s_waitcnt vmcnt(6)\n"
        "ds_read_b64_tr_b16 v[128:129], v44 offset:8192\n"
        "ds_read_b64_tr_b16 v[130:131], v44 offset:8320\n"
        "ds_read_b64_tr_b16 v[132:133], v44 offset:9216\n"
        "ds_read_b64_tr_b16 v[134:135], v44 offset:9344\n"
        "s_lshl_b32 s42, s31, 12\n"
        "s_lshl_b32 s43, s30, 9\n"
        "s_add_u32 s42, s42, s43\n"
        "s_lshl_b32 s43, s29, 7\n"
        "s_add_u32 s42, s42, s43\n"
        "s_add_u32 s42, s42, 331776\n"
        "v_lshl_add_u32 v45, v4, 3, s42\n"
        "v_mov_b32 v47, s38\n"
        "s_waitcnt lgkmcnt(0)\n"
        "v_mfma_f32_16x16x32_bf16 v[52:55], v[24:27], v[128:131], 0\n"
        "v_mfma_f32_16x16x32_bf16 v[52:55], v[28:31], v[132:135], v[52:55]\n"
        "v_xor_b32 v44, 32, v7\n"
        "s_nop 6\n"
        "v_fma_f32 v46, -v14, v53, v52\n"
        "v_mul_f32 v46, v15, v46\n"
        "s_mov_b64 exec, 0xffff\n"
        "global_store_dwordx2 v45, v[46:47], s[4:5] sc1\n"
        "s_mov_b64 exec, -1\n"
        "v_and_b32 v93, 31, v1\n"
        "v_cmp_gt_u32 vcc, 24, v93\n"
        "v_lshrrev_b32 v94, 5, v1\n"
        "s_lshl_b32 s42, s37, 12\n"
        "s_add_u32 s42, s42, 331776\n"
        "v_cndmask_b32 v45, 0, v93, vcc\n"
        "v_lshlrev_b32 v45, 4, v45\n"
        "v_lshl_or_b32 v45, v94, 9, v45\n"
        "v_add_u32 v92, s42, v45\n"
        "v_lshlrev_b32 v52, 3, v6\n"
        "s_lshl_b32 s42, s29, 5\n"
        "v_or_b32 v52, s42, v52\n"
        "v_and_b32 v53, 2, v5\n"
        "v_lshlrev_b32 v53, 4, v53\n"
        "v_and_b32 v54, 16, v1\n"
        "v_lshl_or_b32 v53, v54, 2, v53\n"
        "v_xor_b32 v52, v52, v53\n"
        "v_lshl_or_b32 v52, v5, 7, v52\n"
        "v_lshl_or_b32 v95, v3, 6, v52\n"
        "v_lshl_or_b32 v91, v54, 6, v52\n"
        "s_lshl_b32 s42, s31, 3\n"
        "s_add_u32 s42, s42, s30\n"
        "s_lshl_b32 s42, s42, 10\n"
        "s_lshl_b32 s43, s29, 8\n"
        "s_add_u32 s42, s42, s43\n"
        "s_add_u32 s42, s42, 462848\n"
        "v_lshlrev_b32 v47, 3, v94\n"
        "v_lshl_or_b32 v47, v4, 4, v47\n"
        "v_add_u32 v64, s42, v47\n"
        "s_lshl_b32 s42, s40, 5\n"
        "s_add_u32 s42, s42, s37\n"
        "s_lshl_b32 s44, s42, 10\n"
        "s_add_u32 s44, s44, s43\n"
        "s_add_u32 s44, s44, 724992\n"
        "v_lshl_add_u32 v65, v4, 4, s44\n"
        "s_lshl_b32 s42, s42, 2\n"
        "v_mov_b32 v47, s42\n"
        "global_load_dword v66, v47, s[2:3]\n"
        "s_add_u32 s24, s24, s42\n"
        "s_addc_u32 s25, s25, 0\n"
        "s_lshl_b32 s42, s37, 13\n"
        "s_add_u32 s42, s42, 462848\n"
        "s_add_u32 s50, s4, s42\n"
        "s_addc_u32 s51, s5, 0\n"
        "v_mul_u32_u24 v47, 144, v4\n"
        "v_add_u32 v67, v47, v3\n"
        "v_lshrrev_b32 v68, 1, v3\n"
        "v_mov_b32 v74, s38\n"
        "v_mov_b32 v75, s39\n"
        "v_mov_b32 v71, s38\n"
        "s_lshl_b32 s42, s40, 15\n"
        "s_lshl_b32 s43, s37, 5\n"
        "s_add_u32 s42, s42, s43\n"
        "s_add_u32 s42, s42, 724992\n"
        "v_lshrrev_b32 v46, 1, v1\n"
        "v_and_b32 v47, 1, v1\n"
        "v_lshlrev_b32 v45, 4, v47\n"
        "v_lshl_or_b32 v46, v46, 10, v45\n"
        "v_add_u32 v69, s42, v46\n"
        "v_xor_b32 v45, 16, v45\n"
        "s_waitcnt vmcnt(4)\n"
        "ds_read_b64_tr_b16 v[192:193], v7 offset:16384\n"
        "ds_read_b64_tr_b16 v[194:195], v7 offset:16640\n"
        "ds_read_b64_tr_b16 v[196:197], v7 offset:18432\n"
        "ds_read_b64_tr_b16 v[198:199], v7 offset:18688\n"
        "ds_read_b64_tr_b16 v[200:201], v44 offset:16384\n"
        "ds_read_b64_tr_b16 v[202:203], v44 offset:16640\n"
        "ds_read_b64_tr_b16 v[204:205], v44 offset:18432\n"
        "ds_read_b64_tr_b16 v[206:207], v44 offset:18688\n"
        "TAIL:\n"
        "v_mov_b64 v[120:121], 0\n"
        "v_mov_b64 v[122:123], 0\n"
        "v_mov_b64 v[124:125], 0\n"
        "v_mov_b64 v[126:127], 0\n"
        "s_mov_b32 s41, 0\n"
        "s_mov_b32 s18, 0x00ff00ff\n"
        "s_mov_b32 s19, 0xff00ff00\n"
        "s_waitcnt lgkmcnt(0)\n"
        "s_barrier\n"
        "s_sleep 24\n"
        "global_load_dwordx4 v[104:107], v35, s[4:5] sc1\n"
        "global_load_dwordx4 v[96:99], v34, s[4:5] sc1\n"
        "global_load_dwordx4 v[108:111], v35, s[72:73] sc1\n"
        "global_load_dwordx4 v[100:103], v34, s[72:73] sc1\n"
        "H3_LOOP:\n"
        "s_waitcnt vmcnt(0)\n"
        "v_add3_u32 v136, v96, v97, v98\n"
        "v_add3_u32 v137, v100, v101, v102\n"
        "v_max_f32 v138, v104, v108\n"
        "v_add_u32 v136, v136, v99\n"
        "v_add_u32 v137, v137, v103\n"
        "v_max_f32_dpp v138, v138, v138 row_ror:8 row_mask:0xf bank_mask:0xf\n"
        "v_add_u32_dpp v136, v136, v136 quad_perm:[1,0,3,2] row_mask:0xf bank_mask:0xf\n"
        "v_add_u32_dpp v137, v137, v137 quad_perm:[1,0,3,2] row_mask:0xf bank_mask:0xf\n"
        "v_mov_b32 v139, v138\n"
        "v_add_u32_dpp v136, v136, v136 quad_perm:[2,3,0,1] row_mask:0xf bank_mask:0xf\n"
        "v_add_u32_dpp v137, v137, v137 quad_perm:[2,3,0,1] row_mask:0xf bank_mask:0xf\n"
        "v_permlane16_swap_b32 v138, v139\n"
        "v_add_u32_dpp v136, v136, v136 row_half_mirror row_mask:0xf bank_mask:0xf\n"
        "v_add_u32_dpp v137, v137, v137 row_half_mirror row_mask:0xf bank_mask:0xf\n"
        "v_max_f32 v138, v138, v139\n"
        "v_mov_b32 v139, v138\n"
        "v_add3_u32 v136, v136, v104, v105\n"
        "v_add3_u32 v137, v137, v108, v109\n"
        "v_permlane32_swap_b32 v138, v139\n"
        "v_xor_b32 v136, v136, v106\n"
        "v_xor_b32 v137, v137, v110\n"
        "v_max_f32 v138, v138, v139\n"
        "v_bitop3_b32 v136, v136, v107, s38 bitop3:0xf6\n"
        "v_bitop3_b32 v137, v137, v111, s38 bitop3:0xf6\n"
        "v_sub_f32 v140, v104, v138\n"
        "v_sub_f32 v141, v108, v138\n"
        "v_cmp_ne_u32 s[52:53], 0, v136\n"
        "v_cmp_ne_u32 s[54:55], 0, v137\n"
        "v_exp_f32 v140, v140\n"
        "v_exp_f32 v141, v141\n"
        "s_or_b64 s[26:27], s[52:53], s[54:55]\n"
        "s_cbranch_scc0 H3_DONE\n"
        "s_mov_b64 exec, s[52:53]\n"
        "global_load_dwordx4 v[104:107], v35, s[4:5] sc1\n"
        "global_load_dwordx4 v[96:99], v34, s[4:5] sc1\n"
        "s_mov_b64 exec, s[54:55]\n"
        "global_load_dwordx4 v[108:111], v35, s[72:73] sc1\n"
        "global_load_dwordx4 v[100:103], v34, s[72:73] sc1\n"
        "s_mov_b64 exec, -1\n"
        "s_add_u32 s41, s41, 1\n"
        "s_cmp_lt_u32 s41, 32768\n"
        "s_cbranch_scc1 H3_LOOP\n"
        "s_waitcnt vmcnt(0)\n"
        "H3_DONE:\n"
        "v_cvt_pk_bf16_f32 v140, v140, v140\n"
        "v_cvt_pk_bf16_f32 v141, v141, v141\n"
        "v_and_b32 v112, v140, v40\n"
        "v_and_b32 v113, v140, v41\n"
        "v_and_b32 v114, v140, v42\n"
        "v_and_b32 v115, v140, v43\n"
        "v_mov_b32 v120, v105\n"
        "v_mov_b32 v124, v109\n"
        "v_mfma_f32_16x16x32_bf16 v[128:131], v[112:115], v[96:99], 0\n"
        "v_and_b32 v116, v141, v40\n"
        "v_and_b32 v117, v141, v41\n"
        "v_and_b32 v118, v141, v42\n"
        "v_and_b32 v119, v141, v43\n"
        "v_mfma_f32_16x16x32_bf16 v[132:135], v[112:115], v[120:123], 0\n"
        "ds_write_b32 v38, v138 offset:768\n"
        "v_mfma_f32_16x16x32_bf16 v[128:131], v[116:119], v[100:103], v[128:131]\n"
        "v_mfma_f32_16x16x32_bf16 v[132:135], v[116:119], v[124:127], v[132:135]\n"
        "s_mov_b64 exec, s[18:19]\n"
        "s_cmp_ge_u32 s28, 4\n"
        "s_nop 4\n"
        "v_cvt_pk_bf16_f32 v128, v128, v129\n"
        "v_cvt_pk_bf16_f32 v129, v130, v131\n"
        "v_add_f32 v132, v132, v133\n"
        "ds_write_b64 v36, v[128:129] offset:1536\n"
        "ds_write_b32 v37, v132 offset:832\n"
        "s_mov_b64 exec, -1\n"
        "s_cbranch_scc0 XB\n"
        "global_load_dwordx4 v[232:235], v92, s[4:5] offset:0 sc1\n"
        "global_load_dwordx4 v[236:239], v92, s[4:5] offset:1024 sc1\n"
        "global_load_dwordx4 v[240:243], v92, s[4:5] offset:2048 sc1\n"
        "global_load_dwordx4 v[244:247], v92, s[4:5] offset:3072 sc1\n"
        "XB:\n"
        "s_waitcnt lgkmcnt(0)\n"
        "s_barrier\n"
        "s_cbranch_scc1 T2\n"
        "s_add_u32 m0, s46, 32768\n"
        "s_nop 0\n"
        "global_load_lds_dwordx4 v48, s[82:83]\n"
        "s_add_u32 m0, m0, 0x400\n"
        "s_nop 0\n"
        "global_load_lds_dwordx4 v49, s[82:83]\n"
        "s_add_u32 m0, m0, 0x400\n"
        "s_nop 0\n"
        "global_load_lds_dwordx4 v50, s[82:83]\n"
        "s_add_u32 m0, m0, 0x400\n"
        "s_nop 0\n"
        "global_load_lds_dwordx4 v51, s[82:83]\n"
        "s_waitcnt vmcnt(0)\n"
        "s_endpgm\n"
        "T2:\n"
        "ds_read_b128 v[96:99], v67 offset:1536\n"
        "ds_read_b128 v[100:103], v67 offset:1600\n"
        "ds_read_b128 v[104:107], v12 offset:768\n"
        "ds_read_b128 v[108:111], v12 offset:784\n"
        "ds_read_b64 v[112:113], v68 offset:768\n"
        "ds_read_b128 v[116:119], v3 offset:832\n"
        "s_mov_b32 s26, 0xffff\n"
        "s_mov_b32 s27, 0xffff\n"
        "s_waitcnt lgkmcnt(5)\n"
        "v_mfma_f32_16x16x32_bf16 v[120:123], v[96:99], v[192:195], 0\n"
        "v_mfma_f32_16x16x32_bf16 v[124:127], v[96:99], v[200:203], 0\n"
        "s_waitcnt lgkmcnt(4)\n"
        "v_mfma_f32_16x16x32_bf16 v[120:123], v[100:103], v[196:199], v[120:123]\n"
        "v_mfma_f32_16x16x32_bf16 v[124:127], v[100:103], v[204:207], v[124:127]\n"
        "s_waitcnt lgkmcnt(0)\n"
        "v_max3_f32 v104, v104, v105, v106\n"
        "v_max3_f32 v108, v108, v109, v110\n"
        "v_max3_f32 v104, v104, v107, v111\n"
        "v_max_f32 v104, v104, v108\n"
        "v_sub_f32 v112, v112, v104\n"
        "v_sub_f32 v113, v113, v104\n"
        "v_exp_f32 v112, v112\n"
        "v_exp_f32 v113, v113\n"
        "v_add_f32 v116, v116, v117\n"
        "v_add_f32 v118, v118, v119\n"
        "v_mul_f32 v116, v112, v116\n"
        "v_fmac_f32 v116, v113, v118\n"
        "v_mov_b32 v117, v116\n"
        "v_add_f32 v120, v120, v121\n"
        "v_add_f32 v122, v122, v123\n"
        "v_permlane32_swap_b32 v116, v117\n"
        "v_add_f32 v124, v124, v125\n"
        "v_add_f32 v116, v116, v117\n"
        "v_mov_b32 v117, v116\n"
        "v_add_f32 v126, v126, v127\n"
        "v_mul_f32 v120, v112, v120\n"
        "v_permlane16_swap_b32 v116, v117\n"
        "v_mul_f32 v124, v112, v124\n"
        "v_add_f32 v116, v116, v117\n"
        "v_fmac_f32 v120, v113, v122\n"
        "v_fmac_f32 v124, v113, v126\n"
        "v_rcp_f32 v116, v116\n"
        "s_nop 0\n"
        "v_permlane32_swap_b32 v120, v124\n"
        "v_add_f32 v120, v120, v124\n"
        "v_mov_b32 v124, v120\n"
        "s_nop 1\n"
        "v_permlane16_swap_b32 v120, v124\n"
        "v_add_f32 v120, v120, v124\n"
        "v_mul_f32 v70, v120, v116\n"
        "s_mov_b64 exec, s[26:27]\n"
        "global_store_dwordx2 v64, v[70:71], s[4:5] sc1\n"
        "s_mov_b64 exec, -1\n"
        "v_lshlrev_b32 v66, v45, v66\n"
        "v_and_b32 v66, 0xffff0000, v66\n"
        "s_mov_b32 s41, 0\n"
        "s_waitcnt vmcnt(1)\n"
        "HP_LOOP:\n"
        "v_bitop3_b32 v52, v233, v235, s38 bitop3:0x7e\n"
        "v_bitop3_b32 v53, v237, v239, s38 bitop3:0x7e\n"
        "v_bitop3_b32 v54, v241, v243, s38 bitop3:0x7e\n"
        "v_bitop3_b32 v55, v245, v247, s38 bitop3:0x7e\n"
        "v_or3_b32 v52, v52, v53, v54\n"
        "v_or_b32 v52, v52, v55\n"
        "v_cmp_ne_u32 vcc, 0, v52\n"
        "s_cbranch_vccz HP_DONE\n"
        "global_load_dwordx4 v[232:235], v92, s[4:5] offset:0 sc1\n"
        "global_load_dwordx4 v[236:239], v92, s[4:5] offset:1024 sc1\n"
        "global_load_dwordx4 v[240:243], v92, s[4:5] offset:2048 sc1\n"
        "global_load_dwordx4 v[244:247], v92, s[4:5] offset:3072 sc1\n"
        "s_waitcnt vmcnt(0)\n"
        "s_add_u32 s41, s41, 1\n"
        "s_cmp_lt_u32 s41, 32768\n"
        "s_cbranch_scc1 HP_LOOP\n"
        "HP_DONE:\n"
        "v_add_f32 v52, v232, v236\n"
        "v_add_f32 v53, v234, v238\n"
        "v_add_f32 v54, v240, v244\n"
        "v_add_f32 v55, v242, v246\n"
        "v_add_f32 v52, v52, v54\n"
        "v_add_f32 v53, v53, v55\n"
        "s_lshl_b32 s42, s76, 2\n"
        "v_cmp_gt_u32 vcc, s42, v93\n"
        "v_permlane32_swap_b32 v52, v53\n"
        "v_add_f32 v52, v52, v53\n"
        "v_mul_f32 v53, 0xbfb8aa3b, v52\n"
        "v_exp_f32 v53, v53\n"
        "v_lshlrev_b32 v47, 2, v93\n"
        "v_add_f32 v53, 1.0, v53\n"
        "v_rcp_f32 v53, v53\n"
        "v_lshl_or_b32 v47, v94, 1, v47\n"
        "v_mul_f32 v52, v52, v53\n"
        "s_lshl_b32 s42, s29, 7\n"
        "v_add_u32 v47, s42, v47\n"
        "v_cndmask_b32 v52, 0, v52, vcc\n"
        "v_cvt_pk_bf16_f32 v52, v52, v52\n"
        "ds_write_b16 v47, v52 offset:1024\n"
        "v_add_u32 v47, s42, v3\n"
        "ds_read_b128 v[52:55], v47 offset:1024\n"
        "ds_read_b128 v[56:59], v47 offset:1088\n"
        "ds_read_b64_tr_b16 v[224:225], v95 offset:49152\n"
        "ds_read_b64_tr_b16 v[226:227], v95 offset:49664\n"
        "ds_read_b64_tr_b16 v[228:229], v91 offset:53248\n"
        "ds_read_b64_tr_b16 v[230:231], v91 offset:53760\n"
        "s_waitcnt lgkmcnt(0)\n"
        "v_mfma_f32_16x16x32_bf16 v[144:147], v[52:55], v[224:227], 0\n"
        "v_mfma_f32_16x16x32_bf16 v[144:147], v[56:59], v[228:231], v[144:147]\n"
        "s_mov_b32 s41, 0\n"
        "global_load_dwordx4 v[96:99], v32, s[50:51] offset:-2048 sc1\n"
        "global_load_dwordx4 v[100:103], v32, s[50:51] offset:2048 sc1\n"
        "s_sleep 8\n"
        "global_load_dwordx4 v[104:107], v32, s[50:51] offset:-2048 sc1\n"
        "global_load_dwordx4 v[108:111], v32, s[50:51] offset:2048 sc1\n"
        "s_sleep 8\n"
        "H4_LOOP:\n"
        "global_load_dwordx4 v[112:115], v32, s[50:51] offset:-2048 sc1\n"
        "global_load_dwordx4 v[116:119], v32, s[50:51] offset:2048 sc1\n"
        "s_waitcnt vmcnt(4)\n"
        "v_bitop3_b32 v122, v97, v99, s38 bitop3:0x7e\n"
        "v_bitop3_b32 v123, v101, v103, s38 bitop3:0x7e\n"
        "v_or_b32 v122, v122, v123\n"
        "v_add_f32 v120, v96, v100\n"
        "v_add_f32 v121, v98, v102\n"
        "v_cmp_ne_u32 vcc, 0, v122\n"
        "s_cbranch_vccz H4_DONE\n"
        "global_load_dwordx4 v[96:99], v32, s[50:51] offset:-2048 sc1\n"
        "global_load_dwordx4 v[100:103], v32, s[50:51] offset:2048 sc1\n"
        "s_waitcnt vmcnt(4)\n"
        "v_bitop3_b32 v122, v105, v107, s38 bitop3:0x7e\n"
        "v_bitop3_b32 v123, v109, v111, s38 bitop3:0x7e\n"
        "v_or_b32 v122, v122, v123\n"
        "v_add_f32 v120, v104, v108\n"
        "v_add_f32 v121, v106, v110\n"
        "v_cmp_ne_u32 vcc, 0, v122\n"
        "s_cbranch_vccz H4_DONE\n"
        "global_load_dwordx4 v[104:107], v32, s[50:51] offset:-2048 sc1\n"
        "global_load_dwordx4 v[108:111], v32, s[50:51] offset:2048 sc1\n"
        "s_waitcnt vmcnt(4)\n"
        "v_bitop3_b32 v122, v113, v115, s38 bitop3:0x7e\n"
        "v_bitop3_b32 v123, v117, v119, s38 bitop3:0x7e\n"
        "v_or_b32 v122, v122, v123\n"
        "v_add_f32 v120, v112, v116\n"
        "v_add_f32 v121, v114, v118\n"
        "v_cmp_ne_u32 vcc, 0, v122\n"
        "s_cbranch_vccz H4_DONE\n"
        "s_add_u32 s41, s41, 1\n"
        "s_cmp_lt_u32 s41, 32768\n"
        "s_cbranch_scc1 H4_LOOP\n"
        "H4_DONE:\n"
        "v_permlane32_swap_b32 v120, v121\n"
        "v_add_f32 v120, v120, v121\n"
        "v_mov_b32 v121, v120\n"
        "s_nop 1\n"
        "v_permlane16_swap_b32 v120, v121\n"
        "v_add_f32 v120, v120, v121\n"
        "v_cvt_pk_bf16_f32 v120, v120, v120\n"
        "ds_write_b16 v33, v120 offset:512\n"
        "s_waitcnt lgkmcnt(0)\n"
        "s_barrier\n"
        "ds_read_b128 v[128:131], v3 offset:512\n"
        "ds_read_b64_tr_b16 v[208:209], v95 offset:32768\n"
        "ds_read_b64_tr_b16 v[210:211], v95 offset:33280\n"
        "ds_read_b128 v[132:135], v3 offset:576\n"
        "ds_read_b64_tr_b16 v[212:213], v95 offset:36864\n"
        "ds_read_b64_tr_b16 v[214:215], v95 offset:37376\n"
        "ds_read_b128 v[136:139], v3 offset:640\n"
        "ds_read_b64_tr_b16 v[216:217], v95 offset:40960\n"
        "ds_read_b64_tr_b16 v[218:219], v95 offset:41472\n"
        "ds_read_b128 v[140:143], v3 offset:704\n"
        "ds_read_b64_tr_b16 v[220:221], v95 offset:45056\n"
        "ds_read_b64_tr_b16 v[222:223], v95 offset:45568\n"
        "v_mov_b32 v73, v144\n"
        "s_waitcnt lgkmcnt(9)\n"
        "v_mfma_f32_16x16x32_bf16 v[48:51], v[128:131], v[208:211], 0\n"
        "s_waitcnt lgkmcnt(6)\n"
        "v_mfma_f32_16x16x32_bf16 v[48:51], v[132:135], v[212:215], v[48:51]\n"
        "s_waitcnt lgkmcnt(3)\n"
        "v_mfma_f32_16x16x32_bf16 v[48:51], v[136:139], v[216:219], v[48:51]\n"
        "s_waitcnt lgkmcnt(0)\n"
        "v_mfma_f32_16x16x32_bf16 v[48:51], v[140:143], v[220:223], v[48:51]\n"
        "s_mov_b64 exec, 0xffff\n"
        "s_cmp_eq_u32 s29, 0\n"
        "s_nop 5\n"
        "v_mov_b32 v72, v48\n"
        "global_store_dwordx4 v65, v[72:75], s[4:5] sc1\n"
        "s_mov_b64 exec, -1\n"
        "s_cbranch_scc1 H5\n"
        "s_endpgm\n"
        "H5:\n"
        "s_mov_b32 s41, 0\n"
        "s_sleep 4\n"
        "global_load_dwordx4 v[76:79], v69, s[4:5] sc1\n"
        "s_sleep 8\n"
        "global_load_dwordx4 v[80:83], v69, s[4:5] sc1\n"
        "s_sleep 8\n"
        "H5_LOOP:\n"
        "global_load_dwordx4 v[84:87], v69, s[4:5] sc1\n"
        "s_waitcnt vmcnt(2)\n"
        "v_cmp_ne_u64 vcc, s[38:39], v[78:79]\n"
        "v_mov_b64 v[52:53], v[76:77]\n"
        "s_cbranch_vccz H5_DONE\n"
        "global_load_dwordx4 v[76:79], v69, s[4:5] sc1\n"
        "s_waitcnt vmcnt(2)\n"
        "v_cmp_ne_u64 vcc, s[38:39], v[82:83]\n"
        "v_mov_b64 v[52:53], v[80:81]\n"
        "s_cbranch_vccz H5_DONE\n"
        "global_load_dwordx4 v[80:83], v69, s[4:5] sc1\n"
        "s_waitcnt vmcnt(2)\n"
        "v_cmp_ne_u64 vcc, s[38:39], v[86:87]\n"
        "v_mov_b64 v[52:53], v[84:85]\n"
        "s_cbranch_vccz H5_DONE\n"
        "s_add_u32 s41, s41, 1\n"
        "s_cmp_lt_u32 s41, 32768\n"
        "s_cbranch_scc1 H5_LOOP\n"
        "H5_DONE:\n"
        "s_nop 0\n"
        "v_permlane32_swap_b32 v52, v53\n"
        "v_add_f32 v52, v52, v53\n"
        "v_mov_b32 v53, v52\n"
        "s_nop 1\n"
        "v_permlane16_swap_b32 v52, v53\n"
        "v_add_f32 v52, v52, v53\n"
        "s_nop 1\n"
        "v_add_f32_dpp v52, v52, v52 row_ror:8 row_mask:0xf bank_mask:0xf\n"
        "s_nop 1\n"
        "v_add_f32_dpp v52, v52, v52 row_ror:4 row_mask:0xf bank_mask:0xf\n"
        "s_nop 1\n"
        "v_add_f32_dpp v52, v52, v52 row_ror:2 row_mask:0xf bank_mask:0xf\n"
        "v_mov_b32 v53, v52\n"
        "s_nop 1\n"
        "v_permlane32_swap_b32 v52, v53\n"
        "v_mul_f32 v54, 0xbfb8aa3b, v53\n"
        "v_exp_f32 v54, v54\n"
        "v_mul_f32 v52, v52, v53\n"
        "v_add_f32 v54, 1.0, v54\n"
        "v_rcp_f32 v54, v54\n"
        "s_cmp_lg_u32 s74, 0\n"
        "v_fmac_f32 v66, v52, v54\n"
        "s_nop 1\n"
        "v_mov_b32_dpp v53, v66 quad_perm:[1,0,3,2] row_mask:0xf bank_mask:0xf\n"
        "s_mov_b64 exec, 1\n"
        "v_cvt_pk_bf16_f32 v53, v66, v53\n"
        "global_store_dword v12, v53, s[24:25]\n"
        "s_cbranch_scc1 FIN\n"
        "global_store_dword v12, v71, s[4:5] sc1\n"
        "FIN:\n"
        "s_mov_b64 exec, -1\n"
        "s_endpgm\n"
        ".rept 256\n"
        "s_nop 0\n"
        ".endr\n"
        :: "s"(__builtin_amdgcn_kernarg_segment_ptr()), "s"(blockIdx.x), "v"(threadIdx.x)
        : "v1", "v2", "v3", "v4", "v5", "v6", "v7", "v8", "v9", "v10", "v11", "v12", "v13", "v14", "v15", "v16", "v17", "v18", "v19", "v20", "v21", "v22", "v23", "v24", "v25", "v26", "v27", "v28", "v29", "v30", "v31", "v32", "v33", "v34", "v35", "v36", "v37", "v38", "v39", "v40", "v41", "v42", "v43", "v44", "v45", "v46", "v47", "v48", "v49", "v50", "v51", "v52", "v53", "v54", "v55", "v56", "v57", "v58", "v59", "v60", "v61", "v62", "v63", "v64", "v65", "v66", "v67", "v68", "v69", "v70", "v71", "v72", "v73", "v74", "v75", "v76", "v77", "v78", "v79", "v80", "v81", "v82", "v83", "v84", "v85", "v86", "v87", "v88", "v89", "v90", "v91", "v92", "v93", "v94", "v95", "v96", "v97", "v98", "v99", "v100", "v101", "v102", "v103", "v104", "v105", "v106", "v107", "v108", "v109", "v110", "v111", "v112", "v113", "v114", "v115", "v116", "v117", "v118", "v119", "v120", "v121", "v122", "v123", "v124", "v125", "v126", "v127", "v128", "v129", "v130", "v131", "v132", "v133", "v134", "v135", "v136", "v137", "v138", "v139", "v140", "v141", "v142", "v143", "v144", "v145", "v146", "v147", "v148", "v149", "v150", "v151", "v152", "v153", "v154", "v155", "v156", "v157", "v158", "v159", "v160", "v161", "v162", "v163", "v164", "v165", "v166", "v167", "v168", "v169", "v170", "v171", "v172", "v173", "v174", "v175", "v176", "v177", "v178", "v179", "v180", "v181", "v182", "v183", "v184", "v185", "v186", "v187", "v188", "v189", "v190", "v191", "v192", "v193", "v194", "v195", "v196", "v197", "v198", "v199", "v200", "v201", "v202", "v203", "v204", "v205", "v206", "v207", "v208", "v209", "v210", "v211", "v212", "v213", "v214", "v215", "v216", "v217", "v218", "v219", "v220", "v221", "v222", "v223", "v224", "v225", "v226", "v227", "v228", "v229", "v230", "v231", "v232", "v233", "v234", "v235", "v236", "v237", "v238", "v239", "v240", "v241", "v242", "v243", "v244", "v245", "v246", "v247", "v248", "v249", "v250", "v251", "v252", "v253", "v254", "v255", "s3", "s4", "s5", "s6", "s7", "s8", "s9", "s10", "s11", "s12", "s13", "s14", "s15", "s16", "s17", "s18", "s19", "s20", "s21", "s22", "s23", "s24", "s25", "s26", "s27", "s28", "s29", "s30", "s31", "s34", "s35", "s36", "s37", "s38", "s39", "s40", "s41", "s42", "s43", "s44", "s45", "s46", "s47", "s48", "s49", "s50", "s51", "s52", "s53", "s54", "s55", "s56", "s57", "s58", "s59", "s60", "s61", "s62", "s63", "s64", "s65", "s66", "s67", "s68", "s69", "s70", "s71", "s72", "s73", "s74", "s75", "s76", "s77", "s78", "s79", "s80", "s81", "s82", "s83", "s84", "s85", "s86", "s87", "s88", "s89", "s90", "s91", "s92", "s93", "s94", "s95", "s96", "s97", "vcc", "scc", "memory");
}

extern "C" void kernel_launch(void* const* d_in, const int* in_sizes, int n_in, void* d_out, int out_size, void* d_ws, size_t ws_size, hipStream_t stream) {
    (void)in_sizes; (void)n_in; (void)out_size; (void)ws_size;
    (void)hipFuncSetAttribute((const void*)mla_kernel, hipFuncAttributeMaxDynamicSharedMemorySize, 163840);
    mla_kernel<<<256, 512, 163840, stream>>>(d_in[0], d_ws, d_in[2], d_in[1], d_in[3], d_in[4], d_in[7], d_in[5], d_in[8], d_out);
}
```
